```python
import torch

def _softmax(x, axis=-1):
    return torch.softmax(x, dim=axis)

def _gelu(x):
    return torch.nn.functional.gelu(x, approximate='tanh')

def _silu(x):
    return torch.nn.functional.silu(x)

def _sigmoid(x):
    return torch.sigmoid(x)

def _layernorm(x, axis=-1, eps=1e-05):
    m = x.mean(dim=axis, keepdim=True)
    v = x.var(dim=axis, keepdim=True, unbiased=False)
    return (x - m) / torch.sqrt(v + eps)

def _scatter_add(tgt, idx, src, axis=0):
    return tgt.clone().index_add_(axis, idx, src)

def _segment_sum(src, seg, n):
    out = torch.zeros((n,) + src.shape[1:], dtype=src.dtype)
    return out.index_add_(0, seg, src)

def _pad(x, pw):
    flat = tuple((v for pair in reversed(pw) for v in pair))
    return torch.nn.functional.pad(x, flat)
kernel_name = 'adaLN_cond__memory_aug_recurrent_mem_tr__dense_activation_gelu__prompt'

def setup_inputs():

    _w = lambda a: (lambda t: t.to(torch.bfloat16) if t.is_floating_point() else t)(torch.as_tensor(a))

    def _A_setup():
        B, S, Dm = (1, 2048, 512)
        H, Dh = (32, 128)
        M, seg = (8, 32)
        seg = min(seg, S)
        S = S // seg * seg
        return {'x': _w((lambda *s: torch.randn(*s, dtype=torch.bfloat16) * 0.05)(B, S, Dm)), 'mem0': _w((lambda *s: torch.randn(*s, dtype=torch.bfloat16) * 0.8)(B, M, Dm)), 'Wq': _w((lambda *s: torch.randn(*s, dtype=torch.bfloat16) * 0.8)(Dm, H, Dh) * Dm ** (-0.5)), 'Wk': _w((lambda *s: torch.randn(*s, dtype=torch.bfloat16) * 0.8)(Dm, H, Dh) * Dm ** (-0.5)), 'Wv': _w((lambda *s: torch.randn(*s, dtype=torch.bfloat16) * 0.8)(Dm, H, Dh) * Dm ** (-0.5)), 'Wo': _w((lambda *s: torch.randn(*s, dtype=torch.bfloat16) * 0.8)(H, Dh, Dm) * (H * Dh) ** (-0.5)), 'seg_len': seg}
    _A_in = _A_setup()

    def _B_setup():
        B, S, D, F = (1, 2048, 512, 1792)
        return {'x': _w((lambda *s: torch.randn(*s, dtype=torch.bfloat16) * 0.05)(B, S, D)), 'w1': _w((lambda *s: torch.randn(*s, dtype=torch.bfloat16) * 0.8)(D, F) * D ** (-0.5)), 'b1': _w((lambda *s: torch.randn(*s, dtype=torch.bfloat16) * 12.8)(F) * 0.01), 'w2': _w((lambda *s: torch.randn(*s, dtype=torch.bfloat16) * 0.4)(F, D) * F ** (-0.5)), 'b2': _w((lambda *s: torch.randn(*s, dtype=torch.bfloat16) * 6.4)(D) * 0.01)}
    _B_in = _B_setup()
    return {'A_x': _A_in['x'], 'A_mem0': _A_in['mem0'], 'A_Wq': _A_in['Wq'], 'A_Wk': _A_in['Wk'], 'A_Wv': _A_in['Wv'], 'A_Wo': _A_in['Wo'], 'A_seg_len': _A_in['seg_len'], 'B_x': _B_in['x'], 'B_w1': _B_in['w1'], 'B_b1': _B_in['b1'], 'B_w2': _B_in['w2'], 'B_b2': _B_in['b2'], 'c': _w((lambda *s: torch.randn(*s, dtype=torch.bfloat16) * 0.1)(1, 512) * 8.0), 'w_mod': _w((lambda *s: torch.randn(*s, dtype=torch.bfloat16) * 0.1)(512, 6 * 512) * 512 ** (-0.5) * 8.0)}

def reference(A_x, A_mem0, A_Wq, A_Wk, A_Wv, A_Wo, A_seg_len, B_x, B_w1, B_b1, B_w2, B_b2, c, w_mod):

    def _A_ref(x, mem0, Wq, Wk, Wv, Wo, seg_len):
        B, S, Dm = x.shape
        H, Dh = (Wq.shape[1], Wq.shape[2])
        M = mem0.shape[1]
        scale = Dh ** (-0.5)
        n_seg = S // seg_len
        L = M + seg_len
        idx = torch.arange(L)
        mask = (idx[:, None] < M) | (idx[:, None] >= idx[None, :])
        mem = mem0
        outs = []
        for s in range(n_seg):
            xs = x[:, s * seg_len:(s + 1) * seg_len, :].clone()
            h = torch.concatenate([mem, xs], axis=1)
            q = torch.einsum('btd,dhe->bhte', h, Wq)
            k = torch.einsum('btd,dhe->bhte', h, Wk)
            v = torch.einsum('btd,dhe->bhte', h, Wv)
            scores = torch.einsum('bhqe,bhke->bhqk', q, k) * scale
            scores = torch.where(mask[None, None], scores, -1000000000.0)
            p = _softmax(scores, axis=-1)
            o = torch.einsum('bhqk,bhke->bhqe', p, v)
            o = torch.einsum('bhte,hed->btd', o, Wo)
            mem = o[:, :M, :].clone()
            outs.append(o[:, M:, :])
        return torch.concatenate(outs, axis=1)

    def _B_ref(x, w1, b1, w2, b2):
        h = torch.einsum('bsd,df->bsf', x, w1) + b1
        h = _gelu(h)
        return torch.einsum('bsf,fd->bsd', h, w2) + b2
    x = A_x
    mod = (_silu(c) @ w_mod).reshape(c.shape[0], 6, 1, -1)
    sa, ca, ga, sb, cb, gb = (mod[:, 0].clone(), mod[:, 1].clone(), mod[:, 2].clone(), mod[:, 3].clone(), mod[:, 4].clone(), mod[:, 5].clone())
    h = _layernorm(x, axis=-1) * (1 + ca) + sa
    x = x + ga * _A_ref(h, A_mem0, A_Wq, A_Wk, A_Wv, A_Wo, A_seg_len)
    h = _layernorm(x, axis=-1) * (1 + cb) + sb
    x = x + gb * _B_ref(h, B_w1, B_b1, B_w2, B_b2)
    return x
```

```cpp
#include <hip/hip_runtime.h>
#include <hip/hip_bf16.h>
#include <stdint.h>

typedef __bf16 bf16x2 __attribute__((ext_vector_type(2)));
typedef __bf16 bf16x8 __attribute__((ext_vector_type(8)));
typedef __bf16 bf16x16 __attribute__((ext_vector_type(16)));
typedef short s16x4 __attribute__((ext_vector_type(4)));
typedef float f32x4 __attribute__((ext_vector_type(4)));
typedef unsigned u32x2 __attribute__((ext_vector_type(2)));
typedef unsigned u32x4 __attribute__((ext_vector_type(4)));
typedef unsigned u32x8 __attribute__((ext_vector_type(8)));
#define LAS __attribute__((address_space(3)))
#define GAS __attribute__((address_space(1)))
typedef LAS char* lds_t;
typedef __amdgpu_buffer_rsrc_t rsrc_t;
#define DEV __device__ __forceinline__
#define SC1 16

#define REC_W1 2
#define REC_PACE "10"
#define REC_W2 5

constexpr int NSEG = 64;
constexpr unsigned MB = 1u << 20;
constexpr unsigned OFF_COMM = 0;
constexpr unsigned OFF_CTRL = 2 * MB;
constexpr unsigned OFF_MOD = OFF_CTRL + 65536;
constexpr unsigned OFF_H1 = 3 * MB;
constexpr unsigned OFF_H2 = 5 * MB;
constexpr unsigned OFF_X1 = 7 * MB;
constexpr unsigned OFF_KVM = 9 * MB;
constexpr unsigned OFF_QKV = 17 * MB;
constexpr unsigned OFF_PA = 65 * MB;
constexpr unsigned OFF_PF = 129 * MB;
constexpr unsigned WS_END = 185 * MB;
constexpr unsigned C_TICKET = 0;
constexpr unsigned C_SPARE = 2048;
constexpr unsigned C_ERR = 2304;
constexpr unsigned C_PAR = 2560;
constexpr unsigned C_LN1 = 4096;
constexpr unsigned C_QKV = 4352;
constexpr unsigned C_REC = 4608;
constexpr unsigned C_ATT = 4864;
constexpr unsigned C_R1 = 5120;
constexpr unsigned C_FFN = 5376;
constexpr unsigned C_ZERO_BYTES = 8192;
constexpr int LDS_BYTES = 160 * 1024;
constexpr unsigned SPIN_CAP = 1u << 19;

struct Params {
  const uint16_t* x; const uint16_t* mem0; const uint16_t* wq; const uint16_t* wk; const uint16_t* wv; const uint16_t* wo;
  const uint16_t* w1; const uint16_t* b1; const uint16_t* w2; const uint16_t* b2; const uint16_t* c; const uint16_t* wmod;
  uint16_t* out; char* ws;
};

DEV float bf2f(unsigned h) { return __builtin_bit_cast(float, h << 16); }
DEV float bflo(unsigned u) { return __builtin_bit_cast(float, u << 16); }
DEV float bfhi(unsigned u) { return __builtin_bit_cast(float, u & 0xffff0000u); }
DEV unsigned pk2(float lo, float hi) { bf16x2 v = {(__bf16)lo, (__bf16)hi}; return __builtin_bit_cast(unsigned, v); }
DEV float rbf(float v) { return (float)(__bf16)v; }
DEV unsigned f2u(float f) { return __builtin_bit_cast(unsigned, f); }
DEV float u2f(unsigned u) { return __builtin_bit_cast(float, u); }
DEV unsigned xcc_id() { return (unsigned)__builtin_amdgcn_s_getreg(20 | (3 << 11)) & 7u; }
DEV char* ws_base(char* ws) { return (char*)(((uintptr_t)ws + 2 * MB - 1) & ~(uintptr_t)(2 * MB - 1)); }
DEV u32x4 ld128(rsrc_t r, unsigned v, unsigned s) { return __builtin_amdgcn_raw_buffer_load_b128(r, v, s, SC1); }
DEV unsigned ld32(rsrc_t r, unsigned v, unsigned s) { return __builtin_amdgcn_raw_buffer_load_b32(r, v, s, SC1); }
DEV void st128(u32x4 d, rsrc_t r, unsigned v, unsigned s) { __builtin_amdgcn_raw_buffer_store_b128(d, r, v, s, SC1); }
DEV void st32(unsigned d, rsrc_t r, unsigned v, unsigned s) { __builtin_amdgcn_raw_buffer_store_b32(d, r, v, s, SC1); }
DEV u32x4 lds128(lds_t p) { return *(LAS u32x4*)p; }
DEV u32x2 ldstr(lds_t p) { return __builtin_bit_cast(u32x2, __builtin_amdgcn_ds_read_tr16_b64_v4i16((LAS s16x4*)p)); }
DEV bf16x8 B8(u32x4 v) { return __builtin_bit_cast(bf16x8, v); }
DEV bf16x16 B16(u32x8 v) { return __builtin_bit_cast(bf16x16, v); }
DEV u32x4 cat2(u32x2 a, u32x2 b) { u32x4 r = {a.x, a.y, b.x, b.y}; return r; }
DEV u32x8 cat4(u32x4 a, u32x4 b) { u32x8 r = {a.x, a.y, a.z, a.w, b.x, b.y, b.z, b.w}; return r; }
DEV f32x4 mfma(u32x4 a, u32x4 b, f32x4 c) { return __builtin_amdgcn_mfma_f32_16x16x32_bf16(B8(a), B8(b), c, 0, 0, 0); }
DEV f32x4 smfmac(u32x4 a, u32x8 b, f32x4 c, int idx) { return __builtin_amdgcn_smfmac_f32_16x16x64_bf16(B8(a), B16(b), c, idx, 0, 0); }
DEV unsigned umax3(unsigned a, unsigned b, unsigned c) { return max(max(a, b), c); }
DEV unsigned umax4(u32x4 a, unsigned m) { return umax3(umax3(a.x, a.y, m), a.z, a.w); }
DEV unsigned nosent(unsigned v) { return min(v, 0xfffffffeu); }
#define VMWAIT(n) asm volatile("s_waitcnt vmcnt(" #n ")" ::: "memory")
#define BARRIER() do { asm volatile("s_waitcnt lgkmcnt(0)\n\ts_barrier" ::: "memory"); } while (0)
#define CFENCE() asm volatile("" ::: "memory")
#define SBAR() __builtin_amdgcn_sched_barrier(0)
#define SMF_A(acc, a, b) asm volatile("v_smfmac_f32_16x16x64_bf16 %0, %1, %2, %3" : "+v"(acc) : "v"(a), "a"(b), "v"(idx))
#define SMF_V(acc, a, b) asm volatile("v_smfmac_f32_16x16x64_bf16 %0, %1, %2, %3" : "+v"(acc) : "v"(a), "v"(b), "v"(idx))
#define MF0(acc, a, b) asm volatile("v_mfma_f32_16x16x32_bf16 %0, %1, %2, 0" : "=&v"(acc) : "v"(a), "v"(b))
#define MF(acc, a, b) asm volatile("v_mfma_f32_16x16x32_bf16 %0, %1, %2, %0" : "+v"(acc) : "v"(a), "v"(b))
#define NOP1(n, x) asm volatile("s_nop " #n : "+v"(x))
#define NOP2(n, x, y) asm volatile("s_nop " #n : "+v"(x), "+v"(y))
#define NOP3(n, x, y, z) asm volatile("s_nop " #n : "+v"(x), "+v"(y), "+v"(z))
#define NOP4(n, x, y, z, u) asm volatile("s_nop " #n : "+v"(x), "+v"(y), "+v"(z), "+v"(u))
#define PIN(x) asm volatile("" : "+v"(x))
DEV unsigned ld32w(rsrc_t r, unsigned v, unsigned so) {
  unsigned d;
  asm volatile("buffer_load_dword %0, %1, %2, %3 offen sc1\n\ts_waitcnt vmcnt(0)" : "=&v"(d) : "v"(v), "s"(r), "s"(so) : "memory");
  return d;
}
#define LD4W(a, b, c, d, v, r, so) asm volatile("buffer_load_dwordx4 %0, %4, %5, %6 offen sc1\n\tbuffer_load_dwordx4 %1, %4, %5, %6 offen offset:1024 sc1\n\t" \
  "buffer_load_dwordx4 %2, %4, %5, %6 offen offset:2048 sc1\n\tbuffer_load_dwordx4 %3, %4, %5, %6 offen offset:3072 sc1\n\ts_waitcnt vmcnt(0)" \
  : "=&v"(a), "=&v"(b), "=&v"(c), "=&v"(d) : "v"(v), "s"(r), "s"(so) : "memory")
DEV float allmax345(float v) {
  float t;
  asm("s_nop 1\n\tv_max_f32_dpp %0, %0, %0 row_ror:8 row_mask:0xf bank_mask:0xf\n\tv_mov_b32 %1, %0\n\ts_nop 1\n\tv_permlane16_swap_b32 %0, %1\n\t"
      "v_max_f32 %0, %0, %1\n\tv_mov_b32 %1, %0\n\ts_nop 1\n\tv_permlane32_swap_b32 %0, %1\n\tv_max_f32 %0, %0, %1" : "+v"(v), "=&v"(t));
  return v;
}
DEV float allsum345(float v) {
  float t;
  asm("s_nop 1\n\tv_add_f32_dpp %0, %0, %0 row_ror:8 row_mask:0xf bank_mask:0xf\n\tv_mov_b32 %1, %0\n\ts_nop 1\n\tv_permlane16_swap_b32 %0, %1\n\t"
      "v_add_f32 %0, %0, %1\n\tv_mov_b32 %1, %0\n\ts_nop 1\n\tv_permlane32_swap_b32 %0, %1\n\tv_add_f32 %0, %0, %1" : "+v"(v), "=&v"(t));
  return v;
}
DEV float fmax3(float a, float b, float c) { float r; asm("v_max3_f32 %0, %1, %2, %3" : "=v"(r) : "v"(a), "v"(b), "v"(c)); return r; }
DEV void glds16(const void* g, unsigned lds_base) {
  unsigned sv;
  asm volatile("s_mov_b32 %0, m0\n\ts_mov_b32 m0, %2\n\ts_nop 0\n\tglobal_load_lds_dwordx4 %1, off\n\ts_mov_b32 m0, %0" : "=&s"(sv) : "v"(g), "s"(lds_base) : "memory");
}
DEV void glds16_sc1(const void* g, unsigned lds_base) {
  unsigned sv;
  asm volatile("s_mov_b32 %0, m0\n\ts_mov_b32 m0, %2\n\ts_nop 0\n\tglobal_load_lds_dwordx4 %1, off sc1\n\ts_mov_b32 m0, %0" : "=&s"(sv) : "v"(g), "s"(lds_base) : "memory");
}
DEV float xor8f(float v) { return u2f((unsigned)__builtin_amdgcn_update_dpp(0, (int)f2u(v), 0x128, 0xf, 0xf, true)); }
DEV float xor4f(float v) { return __shfl_xor(v, 4); }
DEV float add16(float v) { auto r = __builtin_amdgcn_permlane16_swap(f2u(v), f2u(v), false, false); return u2f(r[0]) + u2f(r[1]); }
DEV float add32(float v) { auto r = __builtin_amdgcn_permlane32_swap(f2u(v), f2u(v), false, false); return u2f(r[0]) + u2f(r[1]); }
DEV float max16(float v) { auto r = __builtin_amdgcn_permlane16_swap(f2u(v), f2u(v), false, false); return fmaxf(u2f(r[0]), u2f(r[1])); }
DEV float max32(float v) { auto r = __builtin_amdgcn_permlane32_swap(f2u(v), f2u(v), false, false); return fmaxf(u2f(r[0]), u2f(r[1])); }
DEV float hsum2(float a, float b) { auto r = __builtin_amdgcn_permlane32_swap(f2u(a), f2u(b), false, false); return u2f(r[0]) + u2f(r[1]); }
DEV void nap(int n) { for (int i = 0; i < n; ++i) __builtin_amdgcn_s_sleep(1); }

DEV bool wait_ge(rsrc_t rw, unsigned off, int n, unsigned want, int lane, bool dead) {
  if (dead) return true;
  const unsigned vo = OFF_CTRL + off + 4u * (unsigned)min(lane, n - 1);
  for (unsigned sp = 0;; ++sp) {
    CFENCE();
    const unsigned v = ld32(rw, vo, 0);
    if (__all(v >= want)) return false;
    __builtin_amdgcn_s_sleep(2);
    if ((sp & 255u) == 255u) {
      if (__builtin_amdgcn_readfirstlane(ld32(rw, OFF_CTRL + C_ERR, 0)) != 0u) return true;
      if (sp > SPIN_CAP) { st32(1u, rw, OFF_CTRL + C_ERR, 0); return true; }
    }
  }
}
DEV bool block_wait(rsrc_t rw, unsigned off, int n, unsigned want, int w, int lane, bool dead) {
  if (w == 0) dead = wait_ge(rw, off, n, want, lane, dead);
  BARRIER();
  return dead;
}
DEV void block_publish(rsrc_t rw, unsigned off, unsigned val, int tid) {
  VMWAIT(0);
  BARRIER();
  if (tid == 0) st32(val, rw, OFF_CTRL + off, 0);
}

template <int MODE> DEV unsigned rid_of(unsigned row) {
  if (MODE == 0) return (row & 3u) | (((row >> 3) & 1u) << 2);
  if (MODE == 1) return (row & 1u) | (((row >> 2) & 3u) << 1);
  return ((row >> 1) & 1u) | (((row >> 3) & 1u) << 1);
}
template <int RS, int MODE> DEV void stage_chunk(const char* src, unsigned gstride, unsigned lds_base, int w, int lane) {
  constexpr int CPR = RS / 16;
  constexpr int RPI = 64 / CPR;
#pragma unroll
  for (int i = 0; i < 16; ++i) {
    const unsigned piece = (unsigned)(w * 16 + i);
    const unsigned row = piece * RPI + (unsigned)lane / CPR;
    const unsigned ch = ((unsigned)lane % CPR) ^ (rid_of<MODE>(row) << 1);
    glds16(src + (size_t)row * gstride + ch * 16, lds_base + piece * 1024);
  }
}

__global__ void __launch_bounds__(256) k_setup(Params p) {
  __shared__ float red[4][64];
  __shared__ float sc[512];
  char* wb = ws_base(p.ws);
  const rsrc_t rw = __builtin_amdgcn_make_buffer_rsrc(wb, 0, WS_END, 0x00020000);
  const int b = blockIdx.x, tid = threadIdx.x, lane = tid & 63, w = tid >> 6;
  if (b < 48) {
    const int cg = tid & 7, rr = tid >> 3;
    u32x4 v[16];
#pragma unroll
    for (int i = 0; i < 16; ++i) v[i] = *(const u32x4*)(p.wmod + (size_t)(i * 32 + rr) * 3072 + b * 64 + cg * 8);
    for (int i = tid; i < 512; i += 256) { const float cv = bf2f(p.c[i]); sc[i] = rbf(cv / (1.f + __expf(-cv))); }
    __syncthreads();
    float acc[8] = {};
#pragma unroll
    for (int i = 0; i < 16; ++i) {
      const float s = sc[i * 32 + rr];
      acc[0] += s * bflo(v[i].x); acc[1] += s * bfhi(v[i].x); acc[2] += s * bflo(v[i].y); acc[3] += s * bfhi(v[i].y);
      acc[4] += s * bflo(v[i].z); acc[5] += s * bfhi(v[i].z); acc[6] += s * bflo(v[i].w); acc[7] += s * bfhi(v[i].w);
    }
#pragma unroll
    for (int j = 0; j < 8; ++j) { float t = acc[j]; t += __shfl_xor(t, 8); t = add16(t); t = add32(t); acc[j] = t; }
    if (lane < 8) {
#pragma unroll
      for (int j = 0; j < 8; ++j) red[w][lane * 8 + j] = acc[j];
    }
    __syncthreads();
    if (tid < 64) ((float*)(wb + OFF_MOD))[b * 64 + tid] = rbf(red[0][tid] + red[1][tid] + red[2][tid] + red[3][tid]);
  } else if (b < 48 + 128) {
    const int j = b - 48;
    const u32x4 ones = {~0u, ~0u, ~0u, ~0u};
    if (j == 0) {
#pragma unroll
      for (int i = 0; i < 2; ++i) {
        const int t = tid + 256 * i;
        u32x4 v = *(const u32x4*)(p.mem0 + (t & 7) * 512 + (t >> 3) * 8);
        v.x = nosent(v.x); v.y = nosent(v.y); v.z = nosent(v.z); v.w = nosent(v.w);
        st128(v, rw, OFF_COMM + t * 16, 0); st128(v, rw, OFF_COMM + 8192 + t * 16, 0);
      }
    } else {
#pragma unroll
      for (int i = 0; i < 4; ++i) st128(ones, rw, OFF_COMM + j * 16384 + i * 4096 + tid * 16, 0);
    }
    if (j == 1 && w == 0) {
      VMWAIT(0);
      unsigned best0 = ~0u, best1 = ~0u;
#pragma unroll
      for (int r = 0; r < 4; ++r) {
#pragma unroll
        for (int q = 0; q < 2; ++q) {
          unsigned long long t0, t1;
          asm volatile("s_memtime %0\n\ts_waitcnt lgkmcnt(0)" : "=s"(t0) :: "memory");
          unsigned v = ld32(rw, OFF_COMM + 16384 + q * 8192 + r * 256, 0);
          asm volatile("s_waitcnt vmcnt(0)\n\ts_memtime %0\n\ts_waitcnt lgkmcnt(0)" : "=s"(t1), "+v"(v) :: "memory");
          const unsigned d = (unsigned)(t1 - t0);
          if (q == 0) best0 = min(best0, d); else best1 = min(best1, d);
        }
      }
      st128(ones, rw, OFF_COMM + 16384 + lane * 16, 0);
      st128(ones, rw, OFF_COMM + 16384 + 8192 + lane * 16, 0);
      const unsigned par = (best1 < best0 ? 1u : 0u) ^ (xcc_id() >= 4u ? 1u : 0u);
      if (lane == 0) st32(par, rw, OFF_CTRL + C_PAR, 0);
    }
  } else {
    const u32x4 z = {0, 0, 0, 0};
#pragma unroll
    for (int i = 0; i < 2; ++i) {
      const unsigned o = (unsigned)(tid + 256 * i) * 16;
      if (o != C_PAR) st128(z, rw, OFF_CTRL + o, 0);
    }
  }
}

DEV void role_ln1(const Params& p, char* wb, rsrc_t rw, int a, int tid) {
  const int lane = tid & 63, w = tid >> 6, row = lane & 15, g = lane >> 4;
  const int seg = 2 * a + (w >> 1), tile = w & 1;
  const uint16_t* xr = p.x + (size_t)(seg * 32 + tile * 16 + row) * 512 + g * 8;
  u32x4 v[16];
#pragma unroll
  for (int k = 0; k < 16; ++k) v[k] = *(const u32x4*)(xr + k * 32);
  float s = 0.f;
#pragma unroll
  for (int k = 0; k < 16; ++k) s += (bflo(v[k].x) + bfhi(v[k].x)) + (bflo(v[k].y) + bfhi(v[k].y)) + (bflo(v[k].z) + bfhi(v[k].z)) + (bflo(v[k].w) + bfhi(v[k].w));
  s = add32(add16(s));
  const float mean = s * (1.f / 512.f);
  float q = 0.f;
#pragma unroll
  for (int k = 0; k < 16; ++k) {
#pragma unroll
    for (int j = 0; j < 4; ++j) { const float d0 = bflo(v[k][j]) - mean, d1 = bfhi(v[k][j]) - mean; q += d0 * d0 + d1 * d1; }
  }
  q = add32(add16(q));
  const float rstd = 1.f / sqrtf(q * (1.f / 512.f) + 1e-5f);
  const float* mod = (const float*)(wb + OFF_MOD);
#pragma unroll
  for (int k = 0; k < 16; ++k) {
    const f32x4 s0 = *(const f32x4*)(mod + k * 32 + g * 8), s1 = *(const f32x4*)(mod + k * 32 + g * 8 + 4);
    const f32x4 c0 = *(const f32x4*)(mod + 512 + k * 32 + g * 8), c1 = *(const f32x4*)(mod + 512 + k * 32 + g * 8 + 4);
    u32x4 o;
    o.x = pk2((bflo(v[k].x) - mean) * rstd * (1.f + c0.x) + s0.x, (bfhi(v[k].x) - mean) * rstd * (1.f + c0.y) + s0.y);
    o.y = pk2((bflo(v[k].y) - mean) * rstd * (1.f + c0.z) + s0.z, (bfhi(v[k].y) - mean) * rstd * (1.f + c0.w) + s0.w);
    o.z = pk2((bflo(v[k].z) - mean) * rstd * (1.f + c1.x) + s1.x, (bfhi(v[k].z) - mean) * rstd * (1.f + c1.y) + s1.y);
    o.w = pk2((bflo(v[k].w) - mean) * rstd * (1.f + c1.z) + s1.z, (bfhi(v[k].w) - mean) * rstd * (1.f + c1.w) + s1.w);
    st128(o, rw, OFF_H1 + (unsigned)(((seg * 2 + tile) * 16 + k) * 1024 + lane * 16), 0);
  }
  block_publish(rw, C_LN1 + 4 * a, 1u, tid);
}

DEV void role_qkv(const Params& p, char* wb, rsrc_t rw, int j, lds_t L, int tid) {
  const int lane = tid & 63, w = __builtin_amdgcn_readfirstlane(tid >> 6);
  const int mat = j >> 4, hp = j & 15;
  const uint16_t* W = (mat == 0 ? p.wq : (mat == 1 ? p.wk : p.wv)) + hp * 256;
  const unsigned l0 = (unsigned)(uintptr_t)L;
  u32x4 A[4][16];
  const int gb = lane >> 4, qq = (lane >> 2) & 3, pp = lane & 3;
  const unsigned rid = (unsigned)(qq | ((gb & 1) << 2));
  stage_chunk<512, 0>((const char*)W, 8192, l0, w, lane);
  stage_chunk<512, 0>((const char*)W + (size_t)128 * 8192, 8192, l0 + 65536, w, lane);
#pragma unroll
  for (int ck = 0; ck < 4; ++ck) {
    if (ck < 3) VMWAIT(16); else VMWAIT(0);
    BARRIER();
#pragma unroll
    for (int t = 0; t < 4; ++t) {
      const unsigned chunk = (unsigned)(w * 8 + (t >> 1) * 4 + pp) ^ (rid << 1);
      const lds_t b = L + (ck & 1) * 65536 + (8 * gb + qq) * 512 + chunk * 16 + (t & 1) * 8;
#pragma unroll
      for (int k = 0; k < 4; ++k) A[t][ck * 4 + k] = cat2(ldstr(b + k * 32 * 512), ldstr(b + (k * 32 + 4) * 512));
    }
    BARRIER();
    if (ck < 2) stage_chunk<512, 0>((const char*)W + (size_t)(ck + 2) * 128 * 8192, 8192, l0 + (ck & 1) * 65536, w, lane);
  }
  bool dead = false;
  dead = block_wait(rw, C_LN1, 32, 1u, w, lane, dead);
  const char* h1 = wb + OFF_H1 + w * 8192 + lane * 16;
  auto fetch = [&](int s) {
#pragma unroll
    for (int i = 0; i < 8; ++i) glds16_sc1(h1 + (size_t)s * 32768 + i * 1024, l0 + (s & 3) * 32768 + w * 8192 + i * 1024);
  };
  fetch(0); fetch(1);
  const unsigned obase = OFF_QKV + (unsigned)mat * 16 * MB + (unsigned)(hp * 2 + (w >> 1)) * 8192 + (unsigned)(w & 1) * 2048 + lane * 16;
  for (int s = 0; s < NSEG; ++s) {
    if (s == 0) VMWAIT(8); else VMWAIT(12);
    BARRIER();
    if (s + 2 < NSEG) fetch(s + 2);
    const lds_t hb = L + (s & 3) * 32768 + lane * 16;
    f32x4 acc[2][4] = {};
#pragma unroll
    for (int k = 0; k < 16; ++k) {
      const u32x4 b0 = lds128(hb + k * 1024), b1 = lds128(hb + 16384 + k * 1024);
#pragma unroll
      for (int t = 0; t < 4; ++t) { acc[0][t] = mfma(A[t][k], b0, acc[0][t]); acc[1][t] = mfma(A[t][k], b1, acc[1][t]); }
      if (k == 7 && s > 0) {
        if (s + 2 < NSEG) VMWAIT(8); else VMWAIT(0);
        BARRIER();
        if (tid == 0) st32((unsigned)s, rw, OFF_CTRL + C_QKV + 4 * j, 0);
      }
    }
#pragma unroll
    for (int tt = 0; tt < 2; ++tt)
#pragma unroll
      for (int ks = 0; ks < 2; ++ks) {
        const f32x4 a = acc[tt][2 * ks], b = acc[tt][2 * ks + 1];
        const u32x4 o = {pk2(a.x, a.y), pk2(a.z, a.w), pk2(b.x, b.y), pk2(b.z, b.w)};
        st128(o, rw, obase + tt * 4096 + ks * 1024, (unsigned)s * 262144);
      }
    if (s < 2 || s == NSEG - 1) block_publish(rw, C_QKV + 4 * j, (unsigned)(s + 1), tid);
  }
}

DEV void role_rec(const Params& p, char* wb, rsrc_t rw, int c, lds_t L, int tid) {
  const int lane = tid & 63, w = __builtin_amdgcn_readfirstlane(tid >> 6);
  const int h = c >> 1, a = c & 1;
  const unsigned l0 = (unsigned)(uintptr_t)L;
  const int gb = lane >> 4, qq = (lane >> 2) & 3, pp = lane & 3;
  u32x8 Wq[2][8], Wk[2][8], Wv[2][8];
  constexpr int L_WO = 0, L_KV = 65536, L_EQ = 131072, L_EK = L_EQ + 2048, L_EV = L_EQ + 4096, L_WV = L_EQ + 6144;
  {
    const char* src[3] = {(const char*)(p.wq + h * 128), (const char*)(p.wk + h * 128), (const char*)(p.wv + h * 128)};
    const char* wo = (const char*)(p.wo + (size_t)h * 128 * 512 + a * 256);
    stage_chunk<256, 1>(src[0], 8192, l0 + 65536, w, lane);
    stage_chunk<256, 1>(src[0] + (size_t)256 * 8192, 8192, l0, w, lane);
    const unsigned rowl = (unsigned)(16 * (gb >> 1) + 4 * (gb & 1) + 8 * (qq >> 1) + (qq & 1));
    const unsigned rid = rid_of<1>(rowl);
#pragma unroll
    for (int ck = 0; ck < 6; ++ck) {
      VMWAIT(16);
      BARRIER();
#pragma unroll
      for (int t = 0; t < 2; ++t) {
        const unsigned chunk = (unsigned)(4 * w + 2 * t + (pp >> 1)) ^ (rid << 1);
        const lds_t b = L + ((ck + 1) & 1) * 65536 + rowl * 256 + chunk * 16 + (pp & 1) * 8;
#pragma unroll
        for (int T = 0; T < 4; ++T) {
          const u32x8 f = cat4(cat2(ldstr(b + (64 * T) * 256), ldstr(b + (64 * T + 2) * 256)), cat2(ldstr(b + (64 * T + 32) * 256), ldstr(b + (64 * T + 34) * 256)));
          if (ck < 2) Wq[t][(ck & 1) * 4 + T] = f; else if (ck < 4) Wk[t][(ck & 1) * 4 + T] = f; else Wv[t][(ck & 1) * 4 + T] = f;
        }
      }
      BARRIER();
      if (ck + 2 < 6) stage_chunk<256, 1>(src[(ck + 2) >> 1] + (size_t)(ck & 1) * 256 * 8192, 8192, l0 + ((ck + 1) & 1) * 65536, w, lane);
      else if (ck == 4) stage_chunk<512, 0>(wo, 1024, l0 + 65536, w, lane);
      else CFENCE();
      if (ck == 5) break;
    }
    VMWAIT(0);
    BARRIER();
    const unsigned rid0 = (unsigned)(qq | ((gb & 1) << 2));
#pragma unroll
    for (int t = 0; t < 4; ++t) {
      const unsigned chunk = (unsigned)(8 * w + 2 * t + (pp >> 1)) ^ (rid0 << 1);
      const lds_t b = L + 65536 + (8 * gb + qq) * 512 + chunk * 16 + (pp & 1) * 8;
#pragma unroll
      for (int k = 0; k < 4; ++k)
        *(LAS u32x4*)(L + L_WO + w * 16384 + (t * 4 + k) * 1024 + lane * 16) = cat2(ldstr(b + k * 32 * 512), ldstr(b + (k * 32 + 4) * 512));
    }
    BARRIER();
  }
#pragma unroll
  for (int t = 0; t < 2; ++t)
#pragma unroll
    for (int k = 0; k < 8; ++k) { asm volatile("" : "+a"(Wq[t][k])); asm volatile("" : "+a"(Wk[t][k])); }

  const lds_t wv_l = L + L_WV + w * 6144 + lane * 16;
#pragma unroll
  for (int k = 0; k < 3; ++k) {
    const u32x8 f = Wv[1][k];
    *(LAS u32x4*)(wv_l + k * 2048) = u32x4{f[0], f[1], f[2], f[3]}; *(LAS u32x4*)(wv_l + k * 2048 + 1024) = u32x4{f[4], f[5], f[6], f[7]};
  }
  const unsigned par = __builtin_amdgcn_readfirstlane(ld32(rw, OFF_CTRL + C_PAR, 0)) & 1u;
  const unsigned cm = OFF_COMM + par * 8192;
  const int idx = (lane & 8) ? 0xEEEE : 0x4444;
  const unsigned l16 = (unsigned)lane * 16;
  const unsigned kv_s = OFF_QKV + (unsigned)(1 + (w >> 1)) * 16 * MB + (unsigned)h * 8192 + (unsigned)(w & 1) * 4096;
  const unsigned kv_l = l0 + L_KV + (unsigned)w * 4096;
#define KVDMA(so, ld, imm) asm volatile("s_mov_b32 m0, %3\n\ts_nop 0\n\tbuffer_load_dwordx4 %0, %1, %2 offen offset:" #imm " sc1 lds" :: "v"(l16), "s"(rw), "s"(so), "s"(ld) : "memory")
  bool dead = false;
  auto wait_kv = [&](unsigned want) {
    if (dead) return;
    const unsigned kvflag = OFF_CTRL + C_QKV + 4u * (unsigned)(16 + (h >> 1)) + (l16 & 16u) * 4u;
    for (unsigned sp = 0;; ++sp) {
      if (__all(ld32w(rw, kvflag, 0u) >= want)) return;
      __builtin_amdgcn_s_sleep(1);
      if ((sp & 255u) == 255u) {
        if (__builtin_amdgcn_readfirstlane(ld32w(rw, 0u, OFF_CTRL + C_ERR)) != 0u) { dead = true; return; }
        if (sp > SPIN_CAP) { st32(1u, rw, OFF_CTRL + C_ERR, 0); dead = true; return; }
      }
    }
  };
  wait_kv(1);
  { const unsigned so = kv_s, ld = kv_l; KVDMA(so, ld, 0); KVDMA(so, ld, 1024); KVDMA(so, ld, 2048); KVDMA(so, ld, 3072); }
  wait_kv(2);
  { const unsigned so = kv_s + 262144, ld = kv_l + 16384; KVDMA(so, ld, 0); KVDMA(so, ld, 1024); KVDMA(so, ld, 2048); KVDMA(so, ld, 3072); }
  wait_kv(3);
  VMWAIT(0);
  BARRIER();

  u32x8 U[4];
#pragma unroll
  for (int t = 0; t < 4; ++t) { U[t][3] = 0; U[t][7] = 0; U[t][2] = 0; U[t][6] = 0; }
  const lds_t wo_l = L + L_WO + w * 16384 + l16;
  auto ux_tiles = [&](int s, int t0, int t1) {
    const lds_t vb = L + L_KV + (s & 3) * 16384 + 8192 + l16;
    u32x4 vf[2][4];
#pragma unroll
    for (int k = 0; k < 4; ++k) { vf[0][k] = lds128(vb + k * 1024); vf[1][k] = lds128(vb + 4096 + k * 1024); }
#pragma unroll
    for (int t = t0; t < t1; ++t) {
      f32x4 d0, d1;
      u32x4 b[4];
#pragma unroll
      for (int k = 0; k < 4; ++k) b[k] = lds128(wo_l + (t * 4 + k) * 1024);
      MF0(d0, vf[0][0], b[0]); MF0(d1, vf[1][0], b[0]);
#pragma unroll
      for (int k = 1; k < 4; ++k) { MF(d0, vf[0][k], b[k]); MF(d1, vf[1][k], b[k]); }
      NOP2(7, d0, d1);
      U[t][0] = pk2(d0.x, d0.y); U[t][4] = pk2(d0.z, d0.w); U[t][1] = pk2(d1.x, d1.y); U[t][5] = pk2(d1.z, d1.w);
    }
  };
  ux_tiles(0, 0, 4);

  const int n16 = lane & 15;
  const lds_t e_w = L + L_EQ + (32 * w + 16 * (lane >> 5) + n16) * 16 + (gb & 1) * 8;
  const lds_t ek_w = e_w + 2048 - (gb & 1) * 4;
  const lds_t eq_r = L + L_EQ + (8 * gb + qq) * 16 + (pp & 1) * 8;
  const lds_t ek_r = L + L_EK + (8 * gb + qq) * 16 + (pp >> 1) * 8;
  const lds_t ev_r = L + L_EV + (16 * gb + 4 * (qq >> 1) + (qq & 1) + 2 * (pp >> 1)) * 16 + (pp & 1) * 8;
  const int prow = 8 * ((n16 >> 2) & 1) + 4 * ((n16 >> 1) & 1) + 2 * (n16 >> 3) + (n16 & 1);
  const lds_t kx_r = L + L_KV + (gb * 16 + prow) * 16;
  const int ptile = ((gb & 1) << 1) | (gb >> 1);
  const unsigned rho = (unsigned)(32 * a + 8 * w + 2 * ptile + (n16 >> 3));
  const unsigned part_w = cm + (64 + (rho >> 1)) * 16384 + (rho & 1) * 4096 + (unsigned)h * 128 + (lane & 7) * 16;
  const unsigned inbox = cm + (64 + ((unsigned)c >> 1)) * 16384 + ((unsigned)c & 1) * 4096;
  const float CS = 0.08838834764831845f * 1.4426950408889634f;

  auto publish_kv = [&](int s) {
    const lds_t e = L + (a ? L_EV : L_EK) + l16;
    const unsigned o = OFF_KVM + (unsigned)(h * 4096 + a * 2048) + (unsigned)s * 131072;
    st128(lds128(e), rw, l16, o); st128(lds128(e + 1024), rw, l16 + 1024, o);
  };
  auto flag_kv = [&](int s) {
    VMWAIT(0);
    unsigned ll = l16; PIN(ll);
    st32((unsigned)(s + 1), rw, ll == 0u ? 0u : 0xf0000000u, OFF_CTRL + C_REC + 4 * c);
  };
  for (int s = 0;; ++s) {
    u32x4 m[8];
    f32x4 aq0 = {}, aq1 = {}, ak0 = {}, ak1 = {}, av0 = {}, av1 = {};
    const unsigned ms0 = cm + (unsigned)s * 16384, ms1 = ms0 + 4096;
    const unsigned dso = kv_s + (unsigned)min(s + 2, NSEG - 1) * 262144, dld = kv_l + (unsigned)((s + 2) & 3) * 16384;
#define MLD(T, so, imm) asm volatile("buffer_load_dwordx4 %0, %1, %2, %3 offen offset:" #imm " sc1\n\ts_nop " REC_PACE : "=v"(m[T]) : "v"(l16), "s"(rw), "s"(so) : "memory")
    MLD(0, ms0, 0); MLD(1, ms0, 1024); MLD(2, ms0, 2048); MLD(3, ms0, 3072); MLD(4, ms1, 0); MLD(5, ms1, 1024); MLD(6, ms1, 2048); MLD(7, ms1, 3072);
    NOP4(0, aq0, aq1, ak0, ak1); NOP2(0, av0, av1);
    unsigned mx = 0;
#define QK(T) do { SMF_A(aq0, m[T], Wq[0][T]); SMF_A(aq1, m[T], Wq[1][T]); SMF_A(ak0, m[T], Wk[0][T]); SMF_A(ak1, m[T], Wk[1][T]); } while (0)
#define MW(T, n) asm volatile("s_waitcnt vmcnt(" #n ")" : "+v"(m[T]))
    MW(0, 7); QK(0); mx = umax4(m[0], mx); KVDMA(dso, dld, 0);
    MW(1, 7); QK(1); mx = umax4(m[1], mx); KVDMA(dso, dld, 1024);
    MW(2, 7); QK(2); mx = umax4(m[2], mx); KVDMA(dso, dld, 2048);
    MW(3, 7); QK(3); mx = umax4(m[3], mx); KVDMA(dso, dld, 3072);
    MW(4, 7); QK(4); mx = umax4(m[4], mx);
    MW(5, 6); QK(5); mx = umax4(m[5], mx);
    MW(6, 5); QK(6); mx = umax4(m[6], mx);
    MW(7, 4); mx = umax4(m[7], mx); QK(7);
    if (__builtin_expect(!__all(mx != ~0u) && !dead, 0)) {
      for (unsigned sp = 0;; ++sp) {
        mx = 0;
        MLD(0, ms0, 0); MLD(1, ms0, 1024); MLD(2, ms0, 2048); MLD(3, ms0, 3072); MLD(4, ms1, 0); MLD(5, ms1, 1024); MLD(6, ms1, 2048); MLD(7, ms1, 3072);
        asm volatile("s_waitcnt vmcnt(0)" : "+v"(m[0]), "+v"(m[1]), "+v"(m[2]), "+v"(m[3]), "+v"(m[4]), "+v"(m[5]), "+v"(m[6]), "+v"(m[7]));
#pragma unroll
        for (int T = 0; T < 8; ++T) mx = umax4(m[T], mx);
        if (__all(mx != ~0u)) break;
        if ((sp & 255u) == 255u) {
          if (__builtin_amdgcn_readfirstlane(ld32w(rw, 0u, OFF_CTRL + C_ERR)) != 0u) { dead = true; break; }
          if (sp > SPIN_CAP) { st32(1u, rw, OFF_CTRL + C_ERR, 0); dead = true; break; }
        }
      }
      aq0 = f32x4{}; aq1 = f32x4{}; ak0 = f32x4{}; ak1 = f32x4{};
      NOP4(1, aq0, aq1, ak0, ak1);
      QK(0); QK(1); QK(2); QK(3); QK(4); QK(5); QK(6); QK(7);
    }
#define VV(T) do { SMF_V(av0, m[T], Wv[0][T]); SMF_V(av1, m[T], Wv[1][T]); } while (0)
#define VL(T) do { SMF_V(av0, m[T], Wv[0][T]); const u32x8 f_ = cat4(lds128(wv_l + T * 2048), lds128(wv_l + T * 2048 + 1024)); SMF_V(av1, m[T], f_); } while (0)
    VL(0); VL(1);
    NOP2(1, aq0, aq1);
    {
      u32x2 qv = {pk2(hsum2(aq0.x, aq1.x), hsum2(aq0.y, aq1.y)), pk2(hsum2(aq0.z, aq1.z), hsum2(aq0.w, aq1.w))};
      *(LAS u32x2*)e_w = qv;
    }
    VL(2);
    PIN(ak0); PIN(ak1);
    *(LAS unsigned*)ek_w = pk2(hsum2(ak0.x, ak1.x), hsum2(ak0.y, ak1.y));
    *(LAS unsigned*)(ek_w + 8) = pk2(hsum2(ak0.z, ak1.z), hsum2(ak0.w, ak1.w));
    VV(3); VV(4);
    BARRIER();
    const lds_t kb = kx_r + (s & 3) * 16384;
    f32x4 s0, s1, sm;
    {
      u32x4 qf[2], kf[2], x0[2], x1[2];
#pragma unroll
      for (int k = 0; k < 2; ++k) {
        qf[k] = cat2(ldstr(eq_r + k * 512), ldstr(eq_r + k * 512 + 64));
        x0[k] = lds128(kb + k * 1024); x1[k] = lds128(kb + 4096 + k * 1024);
        kf[k] = cat2(ldstr(ek_r + k * 512), ldstr(ek_r + k * 512 + 64));
      }
      VV(5); VV(6); VV(7);
      CFENCE();
      u32x4 qg[2], kg[2], y0[2], y1[2];
#pragma unroll
      for (int k = 0; k < 2; ++k) {
        qg[k] = cat2(ldstr(eq_r + (k + 2) * 512), ldstr(eq_r + (k + 2) * 512 + 64));
        y0[k] = lds128(kb + (k + 2) * 1024); y1[k] = lds128(kb + 4096 + (k + 2) * 1024);
        kg[k] = cat2(ldstr(ek_r + (k + 2) * 512), ldstr(ek_r + (k + 2) * 512 + 64));
      }
      MF0(s0, x0[0], qf[0]); MF0(s1, x1[0], qf[0]); MF0(sm, kf[0], qf[0]);
      MF(s0, x0[1], qf[1]); MF(s1, x1[1], qf[1]); MF(sm, kf[1], qf[1]);
      NOP2(1, av0, av1);
      u32x2 vv = {pk2(hsum2(av0.x, av1.x), hsum2(av0.y, av1.y)), pk2(hsum2(av0.z, av1.z), hsum2(av0.w, av1.w))};
      *(LAS u32x2*)(e_w + 4096) = vv;
      MF(s0, y0[0], qg[0]); MF(s1, y1[0], qg[0]); MF(sm, kg[0], qg[0]);
      MF(s0, y0[1], qg[1]); MF(s1, y1[1], qg[1]); MF(sm, kg[1], qg[1]);
    }
    u32x8 wb0 = cat4(lds128(wo_l), lds128(wo_l + 1024)), wb1 = cat4(lds128(wo_l + 2048), lds128(wo_l + 3072));
    BARRIER();
    if (s == NSEG - 1) break;
    {
      const u32x4 vf0 = cat2(ldstr(ev_r), ldstr(ev_r + 128)), vf1 = cat2(ldstr(ev_r + 1024), ldstr(ev_r + 1024 + 128));
      u32x8 wb2 = cat4(lds128(wo_l + 4096), lds128(wo_l + 5120)), wb3 = cat4(lds128(wo_l + 6144), lds128(wo_l + 7168));
      f32x4 um0 = {}, um1 = {}, um2 = {}, um3 = {};
      NOP3(5, s0, s1, sm);
      const bool hi8 = idx != 0x4444;
      f32x4 xs;
      xs.x = hi8 ? s1.x : s0.x; xs.y = hi8 ? s1.y : s0.y; xs.z = hi8 ? s1.z : s0.z; xs.w = hi8 ? s1.w : s0.w;
      float mxv = fmax3(fmax3(xs.x, xs.y, xs.z), fmax3(xs.w, sm.x, sm.y), fmax3(sm.z, sm.w, sm.w));
      NOP4(0, um0, um1, um2, um3);
      PIN(mxv);
      SMF_V(um0, vf0, wb0); SMF_V(um0, vf1, wb1);
      CFENCE();
      wb0 = cat4(lds128(wo_l + 8192), lds128(wo_l + 9216)); wb1 = cat4(lds128(wo_l + 10240), lds128(wo_l + 11264));
      mxv = allmax345(mxv);
      PIN(mxv);
      SMF_V(um1, vf0, wb2); SMF_V(um1, vf1, wb3);
      CFENCE();
      wb2 = cat4(lds128(wo_l + 12288), lds128(wo_l + 13312)); wb3 = cat4(lds128(wo_l + 14336), lds128(wo_l + 15360));
      const float nm = -mxv * CS;
      float e0 = __builtin_amdgcn_exp2f(xs.x * CS + nm), e1 = __builtin_amdgcn_exp2f(xs.y * CS + nm), e2 = __builtin_amdgcn_exp2f(xs.z * CS + nm), e3 = __builtin_amdgcn_exp2f(xs.w * CS + nm);
      PIN(e3);
      SMF_V(um2, vf0, wb0); SMF_V(um2, vf1, wb1);
      float f0 = __builtin_amdgcn_exp2f(sm.x * CS + nm), f1 = __builtin_amdgcn_exp2f(sm.y * CS + nm), f2 = __builtin_amdgcn_exp2f(sm.z * CS + nm), f3 = __builtin_amdgcn_exp2f(sm.w * CS + nm);
      PIN(f3);
      SMF_V(um3, vf0, wb2); SMF_V(um3, vf1, wb3);
      PIN(um0); PIN(um1);
      U[0][2] = pk2(um0.x, um0.y); U[0][6] = pk2(um0.z, um0.w); U[1][2] = pk2(um1.x, um1.y); U[1][6] = pk2(um1.z, um1.w);
      float l = (e0 + e1) + (e2 + e3) + 0.25f * ((f0 + f1) + (f2 + f3));
      l = allsum345(l);
      const float il = __builtin_amdgcn_rcpf(l);
      u32x4 pf = {pk2(e0 * il, e1 * il), pk2(f0 * il, f1 * il), pk2(e2 * il, e3 * il), pk2(f2 * il, f3 * il)};
      f32x4 o0 = {}, o1 = {}, o2 = {}, o3 = {};
      NOP3(0, pf, um2, um3);
      U[2][2] = pk2(um2.x, um2.y); U[2][6] = pk2(um2.z, um2.w); U[3][2] = pk2(um3.x, um3.y); U[3][6] = pk2(um3.z, um3.w);
      NOP4(0, o0, o1, o2, o3); NOP4(1, U[0], U[1], U[2], U[3]);
      SMF_V(o0, pf, U[0]); SMF_V(o1, pf, U[1]); SMF_V(o2, pf, U[2]); SMF_V(o3, pf, U[3]);
      NOP2(5, o0, o1);
      const unsigned a0 = pk2(hsum2(o0.x, o1.x), hsum2(o0.y, o1.y)), a1 = pk2(hsum2(o0.z, o1.z), hsum2(o0.w, o1.w));
      NOP2(1, o2, o3);
      const unsigned b0 = pk2(hsum2(o2.x, o3.x), hsum2(o2.y, o3.y)), b1 = pk2(hsum2(o2.z, o3.z), hsum2(o2.w, o3.w));
      const auto r0 = __builtin_amdgcn_permlane16_swap(a0, b0, false, false), r1 = __builtin_amdgcn_permlane16_swap(a1, b1, false, false);
      const u32x4 pv = {nosent(r0[0]), nosent(r1[0]), nosent(r0[1]), nosent(r1[1])};
      st128(pv, rw, part_w, (unsigned)(s & 1) * 32 * 16384);
      CFENCE();
    }
    if (w == 2) publish_kv(s);
    ux_tiles(s + 1, 0, 2);
    u32x4 in0, in1, in2, in3;
    const unsigned ib = inbox + (unsigned)(s & 1) * 32 * 16384;
    if (w == 0) {
      nap(REC_W1);
      asm volatile("buffer_load_dwordx4 %0, %4, %5, %6 offen sc1\n\tbuffer_load_dwordx4 %1, %4, %5, %6 offen offset:1024 sc1\n\t"
                   "buffer_load_dwordx4 %2, %4, %5, %6 offen offset:2048 sc1\n\tbuffer_load_dwordx4 %3, %4, %5, %6 offen offset:3072 sc1"
                   : "=&v"(in0), "=&v"(in1), "=&v"(in2), "=&v"(in3) : "v"(l16), "s"(rw), "s"(ib) : "memory");
    } else {
      asm volatile("" : "=v"(in0), "=v"(in1), "=v"(in2), "=v"(in3));
    }
    ux_tiles(s + 1, 2, 4);
    if (w == 0) {
      u32x4 sel;
      unsigned ll = l16; PIN(ll);
      { const unsigned one = (ll & 16u) ? 0x3f800000u : 0x00003f80u, d = ll & 96u;
        sel.x = d == 0u ? one : 0u; sel.y = d == 32u ? one : 0u; sel.z = d == 64u ? one : 0u; sel.w = d == 96u ? one : 0u; }
      const unsigned mo = ll < 128u ? ll : 0xf0000000u;
      NOP1(1, sel);
      f32x4 d;
      asm volatile("s_waitcnt vmcnt(0)" : "+v"(in0), "+v"(in1), "+v"(in2), "+v"(in3));
      for (unsigned sp = 0;; ++sp) {
        MF0(d, in0, sel); MF(d, in1, sel); MF(d, in2, sel); MF(d, in3, sel);
        const unsigned mi = umax4(in0, umax4(in1, umax4(in2, umax4(in3, 0u))));
        if (__builtin_expect(__all(mi != ~0u) || dead, 1)) break;
        if ((sp & 255u) == 255u) {
          if (__builtin_amdgcn_readfirstlane(ld32w(rw, 0u, OFF_CTRL + C_ERR)) != 0u) dead = true;
          if (sp > SPIN_CAP) { st32(1u, rw, OFF_CTRL + C_ERR, 0); dead = true; }
        }
        LD4W(in0, in1, in2, in3, l16, rw, ib);
      }
      NOP1(7, d);
      const unsigned u0 = pk2(add32(d.x), add32(d.y)), u1 = pk2(add32(d.z), add32(d.w));
      const auto r0 = __builtin_amdgcn_permlane16_swap(u0, u0, false, false), r1 = __builtin_amdgcn_permlane16_swap(u1, u1, false, false);
      const u32x4 mv = {nosent(r0[0]), nosent(r1[0]), nosent(r0[1]), nosent(r1[1])};
      st128(mv, rw, mo, ms0 + 16384 + (unsigned)c * 128);
    } else {
      if (w == 2) flag_kv(s);
      if (w == 3 && s + 3 < NSEG) wait_kv((unsigned)(s + 4));
    }
    BARRIER();
    {
      u32x4 ones = {~0u, ~0u, ~0u, ~0u};
      PIN(ones);
      st128(ones, rw, l16, ib + (unsigned)w * 1024);
    }
    nap(REC_W2);
  }
  if (w == 2) { publish_kv(NSEG - 1); flag_kv(NSEG - 1); }
}

DEV void role_att(const Params& p, char* wb, rsrc_t rw, int h, lds_t L, int tid) {
  const int lane = tid & 63, w = __builtin_amdgcn_readfirstlane(tid >> 6);
  const unsigned l0 = (unsigned)(uintptr_t)L;
  const int gb = lane >> 4, qq = (lane >> 2) & 3, pp = lane & 3, n16 = lane & 15;
  const unsigned rid = (unsigned)(qq | ((gb & 1) << 2));
  u32x4 A[8][4];
  const char* wo = (const char*)(p.wo + (size_t)h * 128 * 512);
  stage_chunk<1024, 0>(wo, 1024, l0, w, lane);
  stage_chunk<1024, 0>(wo + 65536, 1024, l0 + 65536, w, lane);
  VMWAIT(0);
  BARRIER();
#pragma unroll
  for (int t = 0; t < 8; ++t) {
    const unsigned chunk = (unsigned)(w * 16 + (t >> 1) * 4 + pp) ^ (rid << 1);
    const lds_t b = L + (8 * gb + qq) * 1024 + chunk * 16 + (t & 1) * 8;
#pragma unroll
    for (int k = 0; k < 4; ++k) A[t][k] = cat2(ldstr(b + k * 32768), ldstr(b + k * 32768 + 4096));
  }
  BARRIER();
  constexpr int L_V = 0, L_KM = 8192, L_O = 12288;
  const int tt = w & 1, eh = w >> 1;
  const float SC = 0.08838834764831845f, L2E = 1.4426950408889634f;
  bool dead = false;
  const unsigned qkvflag = OFF_CTRL + C_QKV + 4u * (unsigned)(min(lane, 2) * 16 + (h >> 1));
  for (int s = 0; s < NSEG; ++s) {
    if (w == 0 && !dead) {
      for (unsigned sp = 0;; ++sp) {
        CFENCE();
        if (__all(ld32(rw, qkvflag, 0) >= (unsigned)(s + 1))) break;
        __builtin_amdgcn_s_sleep(2);
        if ((sp & 255u) == 255u) {
          if (__builtin_amdgcn_readfirstlane(ld32(rw, OFF_CTRL + C_ERR, 0)) != 0u) { dead = true; break; }
          if (sp > SPIN_CAP) { st32(1u, rw, OFF_CTRL + C_ERR, 0); dead = true; break; }
        }
      }
    }
    BARRIER();
    const unsigned so = (unsigned)s * 262144;
    const unsigned qo = OFF_QKV + (unsigned)h * 8192 + lane * 16;
    u32x4 qf[4], k0[4], k1[4];
#pragma unroll
    for (int k = 0; k < 4; ++k) { qf[k] = ld128(rw, qo + tt * 4096 + k * 1024, so); k0[k] = ld128(rw, qo + 16 * MB + k * 1024, so); k1[k] = ld128(rw, qo + 16 * MB + 4096 + k * 1024, so); }
    {
      const u32x4 va = ld128(rw, qo + 32 * MB + w * 2048, so), vb = ld128(rw, qo + 32 * MB + w * 2048 + 1024, so);
      *(LAS u32x4*)(L + L_V + w * 2048 + lane * 16) = va; *(LAS u32x4*)(L + L_V + w * 2048 + 1024 + lane * 16) = vb;
    }
    f32x4 s0 = {}, s1 = {}, sm = {};
#pragma unroll
    for (int k = 0; k < 4; ++k) { s0 = mfma(k0[k], qf[k], s0); s1 = mfma(k1[k], qf[k], s1); }
    dead = block_wait(rw, C_REC + 8 * h, 2, (unsigned)(s + 1), w, lane, dead);
    const unsigned mo = OFF_KVM + (unsigned)h * 4096;
    u32x4 vm[4];
#pragma unroll
    for (int t = 0; t < 4; ++t) vm[t] = ld128(rw, mo + 2048 + (unsigned)(64 * eh + 32 * (t >> 1) + 8 * (n16 >> 2) + 4 * (t & 1) + (n16 & 3)) * 16, (unsigned)s * 131072);
    if (w < 2) *(LAS u32x4*)(L + L_KM + w * 1024 + lane * 16) = ld128(rw, mo + w * 1024 + lane * 16, (unsigned)s * 131072);
    BARRIER();
    {
      const lds_t kr = L + L_KM + (8 * gb + qq) * 16 + (pp & 1) * 8;
#pragma unroll
      for (int k = 0; k < 4; ++k) sm = mfma(cat2(ldstr(kr + k * 512), ldstr(kr + k * 512 + 64)), qf[k], sm);
    }
    float sc[12];
#pragma unroll
    for (int r = 0; r < 4; ++r) {
      const bool fut = 4 * gb + r > n16;
      sc[r] = (tt == 0 && fut) ? -1e9f : s0[r] * SC;
      sc[4 + r] = (tt == 0 || fut) ? -1e9f : s1[r] * SC;
      sc[8 + r] = gb < 2 ? sm[r] * SC : -INFINITY;
    }
    float mx = sc[0];
#pragma unroll
    for (int r = 1; r < 12; ++r) mx = fmaxf(mx, sc[r]);
    mx = max32(max16(mx));
    float l = 0.f;
#pragma unroll
    for (int r = 0; r < 12; ++r) { sc[r] = __builtin_amdgcn_exp2f((sc[r] - mx) * L2E); l += sc[r]; }
    l = add32(add16(l));
    const float il = __builtin_amdgcn_rcpf(l);
    const u32x4 px = {pk2(sc[0] * il, sc[1] * il), pk2(sc[2] * il, sc[3] * il), pk2(sc[4] * il, sc[5] * il), pk2(sc[6] * il, sc[7] * il)};
    const u32x2 pm = {pk2(sc[8] * il, sc[9] * il), pk2(sc[10] * il, sc[11] * il)};
    f32x4 o[4];
#pragma unroll
    for (int t = 0; t < 4; ++t) {
      const lds_t vr = L + L_V + (2 * eh + (t >> 1)) * 1024 + (pp * 16 + 4 * gb + qq) * 16 + (t & 1) * 8;
      o[t] = mfma(cat2(ldstr(vr), ldstr(vr + 4096)), px, f32x4{});
      const u32x2 av = {(gb & 1) ? vm[t].y : vm[t].x, (gb & 1) ? vm[t].w : vm[t].z};
      o[t] = __builtin_amdgcn_mfma_f32_16x16x16bf16_1k(__builtin_bit_cast(s16x4, av), __builtin_bit_cast(s16x4, pm), o[t], 0, 0, 0);
    }
#pragma unroll
    for (int k = 0; k < 2; ++k) {
      const f32x4 a = o[2 * k], b = o[2 * k + 1];
      const u32x4 ov = {pk2(a.x, a.y), pk2(a.z, a.w), pk2(b.x, b.y), pk2(b.z, b.w)};
      *(LAS u32x4*)(L + L_O + (tt * 4 + 2 * eh + k) * 1024 + lane * 16) = ov;
    }
    BARRIER();
    u32x4 of[2][4];
#pragma unroll
    for (int k = 0; k < 4; ++k) { of[0][k] = lds128(L + L_O + k * 1024 + lane * 16); of[1][k] = lds128(L + L_O + 4096 + k * 1024 + lane * 16); }
#pragma unroll
    for (int ds = 0; ds < 4; ++ds) {
#pragma unroll
      for (int t2 = 0; t2 < 2; ++t2) {
        f32x4 a = {}, b = {};
#pragma unroll
        for (int k = 0; k < 4; ++k) { a = mfma(A[2 * ds][k], of[t2][k], a); b = mfma(A[2 * ds + 1][k], of[t2][k], b); }
        const u32x4 ov = {pk2(a.x, a.y), pk2(a.z, a.w), pk2(b.x, b.y), pk2(b.z, b.w)};
        st128(ov, rw, OFF_PA + (unsigned)(((t2 * 4 + (n16 >> 2)) * 32 + h) * 4096 + (16 * w + 4 * ds + gb) * 64 + (n16 & 3) * 16), (unsigned)s * MB);
      }
    }
    block_publish(rw, C_ATT + 4 * h, (unsigned)(s + 1), tid);
  }
}

template <int NP> DEV void sum_partials(rsrc_t rw, unsigned vo, unsigned so, float (&acc)[8]) {
  u32x4 v[NP];
#pragma unroll
  for (int i = 0; i < NP; ++i) v[i] = ld128(rw, vo + i * 4096, so);
#pragma unroll
  for (int j = 0; j < 8; ++j) acc[j] = 0.f;
#pragma unroll
  for (int i = 0; i < NP; ++i) {
#pragma unroll
    for (int j = 0; j < 4; ++j) { acc[2 * j] += bflo(v[i][j]); acc[2 * j + 1] += bfhi(v[i][j]); }
  }
}
DEV float row_sum(float v, LAS float* red, int w, int lane) {
  v += xor4f(v); v += xor8f(v); v = add32(add16(v));
  if (lane < 4) red[w * 4 + lane] = v;
  BARRIER();
  const int r = lane & 3;
  return (red[r] + red[4 + r]) + (red[8 + r] + red[12 + r]);
}
DEV void role_red1(const Params& p, char* wb, rsrc_t rw, int b, lds_t L, int tid) {
  const int lane = tid & 63, w = __builtin_amdgcn_readfirstlane(tid >> 6);
  const int team = b >> 3, rg = b & 7, dc = tid >> 2, row = rg * 4 + (tid & 3);
  const float* mod = (const float*)(wb + OFF_MOD) + dc * 8;
  float ga[8], cb[8], sb[8];
#pragma unroll
  for (int j = 0; j < 8; ++j) { ga[j] = mod[1024 + j]; sb[j] = mod[1536 + j]; cb[j] = 1.f + mod[2048 + j]; }
  bool dead = false;
  for (int s = team; s < NSEG; s += 2) {
    const u32x4 xv = *(const u32x4*)(p.x + (size_t)(s * 32 + row) * 512 + dc * 8);
    dead = block_wait(rw, C_ATT, 32, (unsigned)(s + 1), w, lane, dead);
    float acc[8];
    sum_partials<32>(rw, OFF_PA + (unsigned)rg * 131072 + tid * 16, (unsigned)s * MB, acc);
    float sum = 0.f;
#pragma unroll
    for (int j = 0; j < 4; ++j) {
      acc[2 * j] = rbf(bflo(xv[j]) + ga[2 * j] * acc[2 * j]); acc[2 * j + 1] = rbf(bfhi(xv[j]) + ga[2 * j + 1] * acc[2 * j + 1]);
      sum += acc[2 * j] + acc[2 * j + 1];
    }
    const u32x4 x1 = {pk2(acc[0], acc[1]), pk2(acc[2], acc[3]), pk2(acc[4], acc[5]), pk2(acc[6], acc[7])};
    st128(x1, rw, OFF_X1 + (unsigned)rg * 4096 + tid * 16, (unsigned)s * 32768);
    const float mean = row_sum(sum, (LAS float*)L, w, lane) * (1.f / 512.f);
    float q = 0.f;
#pragma unroll
    for (int j = 0; j < 8; ++j) { acc[j] -= mean; q += acc[j] * acc[j]; }
    const float rstd = 1.f / sqrtf(row_sum(q, (LAS float*)(L + 64), w, lane) * (1.f / 512.f) + 1e-5f);
#pragma unroll
    for (int j = 0; j < 8; ++j) acc[j] = acc[j] * rstd * cb[j] + sb[j];
    const u32x4 h2 = {pk2(acc[0], acc[1]), pk2(acc[2], acc[3]), pk2(acc[4], acc[5]), pk2(acc[6], acc[7])};
    st128(h2, rw, OFF_H2 + (unsigned)(((row >> 4) * 16 + (dc >> 2)) * 1024 + ((dc & 3) * 16 + (row & 15)) * 16), (unsigned)s * 32768);
    block_publish(rw, C_R1 + 4 * b, (unsigned)(s + 1), tid);
  }
}
DEV void role_red2(const Params& p, char* wb, rsrc_t rw, int b, int tid) {
  const int lane = tid & 63, w = __builtin_amdgcn_readfirstlane(tid >> 6);
  const int team = b >> 3, rg = b & 7, dc = tid >> 2, row = rg * 4 + (tid & 3);
  const float* mod = (const float*)(wb + OFF_MOD) + dc * 8;
  float gb[8], b2[8];
#pragma unroll
  for (int j = 0; j < 8; ++j) { gb[j] = mod[2560 + j]; b2[j] = bf2f(p.b2[dc * 8 + j]); }
  bool dead = false;
  for (int s = team; s < NSEG; s += 2) {
    dead = block_wait(rw, C_FFN, 28, (unsigned)(s + 1), w, lane, dead);
    const u32x4 xv = ld128(rw, OFF_X1 + (unsigned)rg * 4096 + tid * 16, (unsigned)s * 32768);
    float acc[8];
    sum_partials<28>(rw, OFF_PF + (unsigned)rg * (28 * 4096) + tid * 16, (unsigned)s * (8 * 28 * 4096), acc);
#pragma unroll
    for (int j = 0; j < 4; ++j) {
      acc[2 * j] = bflo(xv[j]) + gb[2 * j] * (acc[2 * j] + b2[2 * j]); acc[2 * j + 1] = bfhi(xv[j]) + gb[2 * j + 1] * (acc[2 * j + 1] + b2[2 * j + 1]);
    }
    const u32x4 o = {pk2(acc[0], acc[1]), pk2(acc[2], acc[3]), pk2(acc[4], acc[5]), pk2(acc[6], acc[7])};
    *(u32x4*)(p.out + (size_t)(s * 32 + row) * 512 + dc * 8) = o;
  }
}

DEV float gelu_t(float v) {
  const float u = 0.7978845608028654f * (v + 0.044715f * v * v * v);
  const float e = __builtin_amdgcn_exp2f(u * (2.f * 1.4426950408889634f));
  return v * (1.f - __builtin_amdgcn_rcpf(e + 1.f));
}
DEV void role_ffn(const Params& p, char* wb, rsrc_t rw, int f, lds_t L, int tid) {
  const int lane = tid & 63, w = __builtin_amdgcn_readfirstlane(tid >> 6);
  const unsigned l0 = (unsigned)(uintptr_t)L;
  const int gb = lane >> 4, qq = (lane >> 2) & 3, pp = lane & 3, n16 = lane & 15;
  const int tt = w & 1, ch = w >> 1;
  u32x4 A1[2][16], A2[8][2];
  stage_chunk<128, 2>((const char*)(p.w1 + f * 64), 3584, l0, w, lane);
  stage_chunk<1024, 0>((const char*)(p.w2 + (size_t)f * 64 * 512), 1024, l0 + 65536, w, lane);
  float b1[8];
#pragma unroll
  for (int j = 0; j < 8; ++j) b1[j] = bf2f(p.b1[f * 64 + ch * 32 + gb * 8 + j]);
  VMWAIT(0);
  BARRIER();
  {
    const unsigned rid2 = (unsigned)((qq >> 1) | ((gb & 1) << 1));
    const unsigned chunk = (unsigned)(ch * 4 + pp) ^ (rid2 << 1);
#pragma unroll
    for (int t = 0; t < 2; ++t) {
      const lds_t b = L + (8 * gb + qq) * 128 + chunk * 16 + t * 8;
#pragma unroll
      for (int k = 0; k < 16; ++k) A1[t][k] = cat2(ldstr(b + k * 4096), ldstr(b + k * 4096 + 512));
    }
    const unsigned rid = (unsigned)(qq | ((gb & 1) << 2));
#pragma unroll
    for (int t = 0; t < 8; ++t) {
      const unsigned c2 = (unsigned)(w * 16 + (t >> 1) * 4 + pp) ^ (rid << 1);
      const lds_t b = L + 65536 + (8 * gb + qq) * 1024 + c2 * 16 + (t & 1) * 8;
#pragma unroll
      for (int k = 0; k < 2; ++k) A2[t][k] = cat2(ldstr(b + k * 32768), ldstr(b + k * 32768 + 4096));
    }
  }
  BARRIER();
  bool dead = false;
  for (int s = 0; s < NSEG; ++s) {
    dead = block_wait(rw, C_R1 + 32 * (s & 1), 8, (unsigned)(s + 1), w, lane, dead);
    u32x4 hf[16];
#pragma unroll
    for (int k = 0; k < 16; ++k) hf[k] = ld128(rw, OFF_H2 + (unsigned)(tt * 16384 + k * 1024 + lane * 16), (unsigned)s * 32768);
    f32x4 a = {}, b = {};
#pragma unroll
    for (int k = 0; k < 16; ++k) { a = mfma(A1[0][k], hf[k], a); b = mfma(A1[1][k], hf[k], b); }
    const u32x4 gv = {pk2(gelu_t(rbf(a.x + b1[0])), gelu_t(rbf(a.y + b1[1]))), pk2(gelu_t(rbf(a.z + b1[2])), gelu_t(rbf(a.w + b1[3]))),
                      pk2(gelu_t(rbf(b.x + b1[4])), gelu_t(rbf(b.y + b1[5]))), pk2(gelu_t(rbf(b.z + b1[6])), gelu_t(rbf(b.w + b1[7])))};
    *(LAS u32x4*)(L + (tt * 2 + ch) * 1024 + lane * 16) = gv;
    BARRIER();
    u32x4 gf[2][2];
#pragma unroll
    for (int k = 0; k < 2; ++k) { gf[0][k] = lds128(L + k * 1024 + lane * 16); gf[1][k] = lds128(L + 2048 + k * 1024 + lane * 16); }
#pragma unroll
    for (int ds = 0; ds < 4; ++ds) {
#pragma unroll
      for (int t2 = 0; t2 < 2; ++t2) {
        f32x4 c = {}, d = {};
#pragma unroll
        for (int k = 0; k < 2; ++k) { c = mfma(A2[2 * ds][k], gf[t2][k], c); d = mfma(A2[2 * ds + 1][k], gf[t2][k], d); }
        const u32x4 ov = {pk2(c.x, c.y), pk2(c.z, c.w), pk2(d.x, d.y), pk2(d.z, d.w)};
        st128(ov, rw, OFF_PF + (unsigned)(((t2 * 4 + (n16 >> 2)) * 28 + f) * 4096 + (16 * w + 4 * ds + gb) * 64 + (n16 & 3) * 16), (unsigned)s * (8 * 28 * 4096));
      }
    }
    block_publish(rw, C_FFN + 4 * f, (unsigned)(s + 1), tid);
  }
}

DEV int role_of(unsigned x, unsigned t) {
  if (t >= 32u) return -1;
  if (x < 4u) {
    if (t < 16u) return (int)(x * 16u + t);
    const unsigned a = (t - 16u) * 4u + x;
    return a < 32u ? (int)(64u + a) : -1;
  }
  const unsigned b = t * 4u + (x - 4u);
  return b < 108u ? (int)(96u + b) : -1;
}

__global__ void __launch_bounds__(256, 1) k_main(Params p) {
  extern __shared__ __attribute__((aligned(16))) char smem[];
  const lds_t L = (lds_t)smem;
  char* wb = ws_base(p.ws);
  const rsrc_t rw = __builtin_amdgcn_make_buffer_rsrc(wb, 0, WS_END, 0x00020000);
  const int tid = threadIdx.x;
  if (tid == 0) {
    unsigned* ctrl = (unsigned*)(wb + OFF_CTRL);
    const unsigned x = xcc_id();
    const unsigned t = __hip_atomic_fetch_add(ctrl + (C_TICKET + x * 256) / 4, 1u, __ATOMIC_RELAXED, __HIP_MEMORY_SCOPE_AGENT);
    int role = role_of(x, t);
    if (role < 0) {
      unsigned cnt[8];
      bool ok = false;
      for (unsigned sp = 0; sp < SPIN_CAP; ++sp) {
        unsigned tot = 0;
#pragma unroll
        for (int i = 0; i < 8; ++i) { cnt[i] = __hip_atomic_load(ctrl + (C_TICKET + i * 256) / 4, __ATOMIC_RELAXED, __HIP_MEMORY_SCOPE_AGENT); tot += cnt[i]; }
        if (tot >= gridDim.x) { ok = true; break; }
        __builtin_amdgcn_s_sleep(4);
      }
      if (ok) {
        bool any = false;
#pragma unroll
        for (int i = 0; i < 8; ++i) any |= cnt[i] < 32u;
        if (any) {
          unsigned rank = __hip_atomic_fetch_add(ctrl + C_SPARE / 4, 1u, __ATOMIC_RELAXED, __HIP_MEMORY_SCOPE_AGENT);
#pragma unroll
          for (int i = 0; i < 8; ++i)
            for (unsigned u = cnt[i]; u < 32u; ++u) {
              const int r = role_of((unsigned)i, u);
              if (r >= 0) { if (rank == 0u && role < 0) role = r; --rank; }
            }
        }
      } else {
        __hip_atomic_store(ctrl + C_ERR / 4, 1u, __ATOMIC_RELAXED, __HIP_MEMORY_SCOPE_AGENT);
      }
    }
    *(LAS int*)L = role;
  }
  __syncthreads();
  const int role = __builtin_amdgcn_readfirstlane(*(LAS int*)L);
  __syncthreads();
  if (role < 0) return;
  if (role < 64) role_rec(p, wb, rw, role, L, tid);
  else if (role < 96) {
    role_ln1(p, wb, rw, role - 64, tid);
    if (role < 80) role_red1(p, wb, rw, role - 64, L, tid); else role_red2(p, wb, rw, role - 80, tid);
  }
  else if (role < 144) role_qkv(p, wb, rw, role - 96, L, tid);
  else if (role < 176) role_att(p, wb, rw, role - 144, L, tid);
  else role_ffn(p, wb, rw, role - 176, L, tid);
}

extern "C" void kernel_launch(void* const* d_in, const int* in_sizes, int n_in, void* d_out, int out_size, void* d_ws, size_t ws_size,
                              hipStream_t stream) {
  Params p;
  p.x = (const uint16_t*)d_in[0]; p.mem0 = (const uint16_t*)d_in[1];
  p.wq = (const uint16_t*)d_in[2]; p.wk = (const uint16_t*)d_in[3]; p.wv = (const uint16_t*)d_in[4]; p.wo = (const uint16_t*)d_in[5];
  p.w1 = (const uint16_t*)d_in[8]; p.b1 = (const uint16_t*)d_in[9]; p.w2 = (const uint16_t*)d_in[10]; p.b2 = (const uint16_t*)d_in[11];
  p.c = (const uint16_t*)d_in[12]; p.wmod = (const uint16_t*)d_in[13];
  p.out = (uint16_t*)d_out; p.ws = (char*)d_ws;
  (void)hipFuncSetAttribute((const void*)k_main, hipFuncAttributeMaxDynamicSharedMemorySize, LDS_BYTES);
  k_setup<<<48 + 128 + 1, 256, 0, stream>>>(p);
  k_main<<<256, 256, LDS_BYTES, stream>>>(p);
}
```

```cpp
#include <hip/hip_runtime.h>
#include <hip/hip_bf16.h>
#include <stdint.h>

typedef __bf16 bf16x2 __attribute__((ext_vector_type(2)));
typedef __bf16 bf16x8 __attribute__((ext_vector_type(8)));
typedef __bf16 bf16x16 __attribute__((ext_vector_type(16)));
typedef short s16x4 __attribute__((ext_vector_type(4)));
typedef float f32x4 __attribute__((ext_vector_type(4)));
typedef unsigned u32x2 __attribute__((ext_vector_type(2)));
typedef unsigned u32x4 __attribute__((ext_vector_type(4)));
typedef unsigned u32x8 __attribute__((ext_vector_type(8)));
typedef unsigned u32x16 __attribute__((ext_vector_type(16)));
typedef unsigned u32x32 __attribute__((ext_vector_type(32)));
#define LAS __attribute__((address_space(3)))
#define GAS __attribute__((address_space(1)))
typedef LAS char* lds_t;
typedef __amdgpu_buffer_rsrc_t rsrc_t;
#define DEV __device__ __forceinline__
#define SC1 16

#define REC_W1 2
#define REC_PACE "10"
#define REC_W2 5

#define LATE_Q 100
#define LATE_S 300
#define PROBE_T 170
#define PF_K 0
#define PF_A 12
#define PF_B 10
#ifndef NVOTE
#define NVOTE 5
#endif
#define VOTE_LATE 0
#define WARM_LEAD 8
constexpr int NSEG = 64;
constexpr unsigned MB = 1u << 20;
constexpr unsigned OFF_COMM = 0;
constexpr unsigned OFF_CTRL = 2 * MB;
constexpr unsigned OFF_FLAG = OFF_CTRL + 65536;
constexpr unsigned OFF_MOD = OFF_CTRL + 262144;
constexpr unsigned OFF_STAT = OFF_MOD + 16384;
constexpr unsigned OFF_TS = 3 * MB;
constexpr unsigned OFF_H1 = 4 * MB;
constexpr unsigned OFF_KVM = 8 * MB;
constexpr unsigned OFF_QKV = 24 * MB;
constexpr unsigned OFF_DBG = 120 * MB;
constexpr unsigned OFF_SEG = 124 * MB;
constexpr unsigned S_PF = 524288, S_H2 = 983040, S_X1 = 1015808;
constexpr unsigned WS_END = 252 * MB;
constexpr int NPAGE = 126;
constexpr unsigned C_TICKET = 0;
constexpr unsigned C_SPARE = 2048;
constexpr unsigned C_ERR = 2304;
constexpr unsigned C_PAR = 4096;
constexpr unsigned FS = 256;
constexpr int F_LN1 = 0, F_KV = 64, F_Q = 96, F_R1 = 112, F_REC = 128, F_ATT = 192, F_FFN = 224;
constexpr int LDS_BYTES = 152 * 1024;
constexpr unsigned SPIN_CAP = 1u << 19;

struct Params {
  const uint16_t* x; const uint16_t* mem0; const uint16_t* wq; const uint16_t* wk; const uint16_t* wv; const uint16_t* wo;
  const uint16_t* w1; const uint16_t* b1; const uint16_t* w2; const uint16_t* b2; const uint16_t* c; const uint16_t* wmod;
  uint16_t* out; char* ws;
};

DEV float bf2f(unsigned h) { return __builtin_bit_cast(float, h << 16); }
DEV float bflo(unsigned u) { return __builtin_bit_cast(float, u << 16); }
DEV float bfhi(unsigned u) { return __builtin_bit_cast(float, u & 0xffff0000u); }
DEV unsigned pk2(float lo, float hi) { bf16x2 v = {(__bf16)lo, (__bf16)hi}; return __builtin_bit_cast(unsigned, v); }
DEV float rbf(float v) { return (float)(__bf16)v; }
DEV unsigned f2u(float f) { return __builtin_bit_cast(unsigned, f); }
DEV float u2f(unsigned u) { return __builtin_bit_cast(float, u); }
DEV unsigned xcc_id() { return (unsigned)__builtin_amdgcn_s_getreg(20 | (3 << 11)) & 7u; }
DEV constexpr unsigned hm(unsigned x) { return ((x >> 13) << 14) | (x & 8191u); }
struct Near { unsigned long long p0, ps; unsigned fb; };
DEV unsigned fa(const Near& n, int idx) { return n.fb + hm((unsigned)idx * FS); }
DEV unsigned fa0(int idx) { return OFF_FLAG + hm((unsigned)idx * FS); }
DEV unsigned segbase(const Near& n, int s) { return OFF_SEG + (unsigned)s * 2 * MB + (unsigned)((n.ps >> s) & 1ull) * 8192; }
DEV char* ws_base(char* ws) { return (char*)(((uintptr_t)ws + 2 * MB - 1) & ~(uintptr_t)(2 * MB - 1)); }
DEV u32x4 ld128(rsrc_t r, unsigned v, unsigned s) { return __builtin_amdgcn_raw_buffer_load_b128(r, v, s, SC1); }
DEV unsigned ld32(rsrc_t r, unsigned v, unsigned s) { return __builtin_amdgcn_raw_buffer_load_b32(r, v, s, SC1); }
DEV void st128(u32x4 d, rsrc_t r, unsigned v, unsigned s) { __builtin_amdgcn_raw_buffer_store_b128(d, r, v, s, SC1); }
DEV void st32(unsigned d, rsrc_t r, unsigned v, unsigned s) { __builtin_amdgcn_raw_buffer_store_b32(d, r, v, s, SC1); }
DEV u32x4 lds128(lds_t p) { return *(LAS u32x4*)p; }
DEV u32x2 ldstr(lds_t p) { return __builtin_bit_cast(u32x2, __builtin_amdgcn_ds_read_tr16_b64_v4i16((LAS s16x4*)p)); }
DEV bf16x8 B8(u32x4 v) { return __builtin_bit_cast(bf16x8, v); }
DEV bf16x16 B16(u32x8 v) { return __builtin_bit_cast(bf16x16, v); }
DEV u32x4 cat2(u32x2 a, u32x2 b) { u32x4 r = {a.x, a.y, b.x, b.y}; return r; }
DEV u32x8 cat4(u32x4 a, u32x4 b) { u32x8 r = {a.x, a.y, a.z, a.w, b.x, b.y, b.z, b.w}; return r; }
DEV f32x4 mfma(u32x4 a, u32x4 b, f32x4 c) { return __builtin_amdgcn_mfma_f32_16x16x32_bf16(B8(a), B8(b), c, 0, 0, 0); }
DEV f32x4 smfmac(u32x4 a, u32x8 b, f32x4 c, int idx) { return __builtin_amdgcn_smfmac_f32_16x16x64_bf16(B8(a), B16(b), c, idx, 0, 0); }
DEV unsigned umax3(unsigned a, unsigned b, unsigned c) { return max(max(a, b), c); }
DEV unsigned umax4(u32x4 a, unsigned m) { return umax3(umax3(a.x, a.y, m), a.z, a.w); }
DEV unsigned nosent(unsigned v) { return min(v, 0xfffffffeu); }
#define VMWAIT(n) asm volatile("s_waitcnt vmcnt(" #n ")" ::: "memory")
#define BARRIER() do { asm volatile("s_waitcnt lgkmcnt(0)\n\ts_barrier" ::: "memory"); } while (0)
#define CFENCE() asm volatile("" ::: "memory")
#define SBAR() __builtin_amdgcn_sched_barrier(0)
#define SMF_A(acc, a, b) asm volatile("v_smfmac_f32_16x16x64_bf16 %0, %1, %2, %3" : "+v"(acc) : "v"(a), "a"(b), "v"(idx))
#define SMF_V(acc, a, b) asm volatile("v_smfmac_f32_16x16x64_bf16 %0, %1, %2, %3" : "+v"(acc) : "v"(a), "v"(b), "v"(idx))
#define MF0(acc, a, b) asm volatile("v_mfma_f32_16x16x32_bf16 %0, %1, %2, 0" : "=&v"(acc) : "v"(a), "v"(b))
#define MF(acc, a, b) asm volatile("v_mfma_f32_16x16x32_bf16 %0, %1, %2, %0" : "+v"(acc) : "v"(a), "v"(b))
#define NOP1(n, x) asm volatile("s_nop " #n : "+v"(x))
#define NOP2(n, x, y) asm volatile("s_nop " #n : "+v"(x), "+v"(y))
#define NOP3(n, x, y, z) asm volatile("s_nop " #n : "+v"(x), "+v"(y), "+v"(z))
#define NOP4(n, x, y, z, u) asm volatile("s_nop " #n : "+v"(x), "+v"(y), "+v"(z), "+v"(u))
#define PIN(x) asm volatile("" : "+v"(x))
DEV unsigned ld32w(rsrc_t r, unsigned v, unsigned so) {
  unsigned d;
  asm volatile("buffer_load_dword %0, %1, %2, %3 offen sc1\n\ts_waitcnt vmcnt(0)" : "=&v"(d) : "v"(v), "s"(r), "s"(so) : "memory");
  return d;
}
#define LD4W(a, b, c, d, v, r, so) asm volatile("buffer_load_dwordx4 %0, %4, %5, %6 offen sc1\n\tbuffer_load_dwordx4 %1, %4, %5, %6 offen offset:1024 sc1\n\t" \
  "buffer_load_dwordx4 %2, %4, %5, %6 offen offset:2048 sc1\n\tbuffer_load_dwordx4 %3, %4, %5, %6 offen offset:3072 sc1\n\ts_waitcnt vmcnt(0)" \
  : "=&v"(a), "=&v"(b), "=&v"(c), "=&v"(d) : "v"(v), "s"(r), "s"(so) : "memory")
DEV float allmax345(float v) {
  float t;
  asm("s_nop 1\n\tv_max_f32_dpp %0, %0, %0 row_ror:8 row_mask:0xf bank_mask:0xf\n\tv_mov_b32 %1, %0\n\ts_nop 1\n\tv_permlane16_swap_b32 %0, %1\n\t"
      "v_max_f32 %0, %0, %1\n\tv_mov_b32 %1, %0\n\ts_nop 1\n\tv_permlane32_swap_b32 %0, %1\n\tv_max_f32 %0, %0, %1" : "+v"(v), "=&v"(t));
  return v;
}
DEV float allsum345(float v) {
  float t;
  asm("s_nop 1\n\tv_add_f32_dpp %0, %0, %0 row_ror:8 row_mask:0xf bank_mask:0xf\n\tv_mov_b32 %1, %0\n\ts_nop 1\n\tv_permlane16_swap_b32 %0, %1\n\t"
      "v_add_f32 %0, %0, %1\n\tv_mov_b32 %1, %0\n\ts_nop 1\n\tv_permlane32_swap_b32 %0, %1\n\tv_add_f32 %0, %0, %1" : "+v"(v), "=&v"(t));
  return v;
}
DEV float fmax3(float a, float b, float c) { float r; asm("v_max3_f32 %0, %1, %2, %3" : "=v"(r) : "v"(a), "v"(b), "v"(c)); return r; }
DEV void glds16(const void* g, unsigned lds_base) {
  unsigned sv;
  asm volatile("s_mov_b32 %0, m0\n\ts_mov_b32 m0, %2\n\ts_nop 0\n\tglobal_load_lds_dwordx4 %1, off\n\ts_mov_b32 m0, %0" : "=&s"(sv) : "v"(g), "s"(lds_base) : "memory");
}
DEV void glds16_sc1(const void* g, unsigned lds_base) {
  unsigned sv;
  asm volatile("s_mov_b32 %0, m0\n\ts_mov_b32 m0, %2\n\ts_nop 0\n\tglobal_load_lds_dwordx4 %1, off sc1\n\ts_mov_b32 m0, %0" : "=&s"(sv) : "v"(g), "s"(lds_base) : "memory");
}
DEV float xor8f(float v) { return u2f((unsigned)__builtin_amdgcn_update_dpp(0, (int)f2u(v), 0x128, 0xf, 0xf, true)); }
DEV float xor4f(float v) { return __shfl_xor(v, 4); }
DEV float add16(float v) { auto r = __builtin_amdgcn_permlane16_swap(f2u(v), f2u(v), false, false); return u2f(r[0]) + u2f(r[1]); }
DEV float add32(float v) { auto r = __builtin_amdgcn_permlane32_swap(f2u(v), f2u(v), false, false); return u2f(r[0]) + u2f(r[1]); }
DEV float max16(float v) { auto r = __builtin_amdgcn_permlane16_swap(f2u(v), f2u(v), false, false); return fmaxf(u2f(r[0]), u2f(r[1])); }
DEV float max32(float v) { auto r = __builtin_amdgcn_permlane32_swap(f2u(v), f2u(v), false, false); return fmaxf(u2f(r[0]), u2f(r[1])); }
DEV float hsum2(float a, float b) { auto r = __builtin_amdgcn_permlane32_swap(f2u(a), f2u(b), false, false); return u2f(r[0]) + u2f(r[1]); }
DEV void nap(int n) { for (int i = 0; i < n; ++i) __builtin_amdgcn_s_sleep(1); }

#define STAMP(r, i) do { } while (0)
#define COUNT(r, i, v) do { } while (0)
#define PH(i) do { } while (0)
DEV bool wait_ge(rsrc_t rw, unsigned vo, unsigned want, bool dead) {
  if (dead) return true;
  for (unsigned sp = 0;; ++sp) {
    CFENCE();
    const unsigned v = ld32(rw, vo, 0);
    if (__all(v >= want)) return false;
    __builtin_amdgcn_s_sleep(2);
    if ((sp & 255u) == 255u) {
      if (__builtin_amdgcn_readfirstlane(ld32(rw, OFF_CTRL + C_ERR, 0)) != 0u) return true;
      if (sp > SPIN_CAP) { st32(1u, rw, OFF_CTRL + C_ERR, 0); return true; }
    }
  }
}
DEV bool block_wait(rsrc_t rw, unsigned vo, unsigned want, int w, bool dead) {
  if (w == 0) dead = wait_ge(rw, vo, want, dead);
  BARRIER();
  return dead;
}
DEV void block_publish(rsrc_t rw, unsigned off, unsigned val, int tid) {
  VMWAIT(0);
  BARRIER();
  if (tid == 0) st32(val, rw, off, 0);
}

template <int MODE> DEV unsigned rid_of(unsigned row) {
  if (MODE == 0) return (row & 3u) | (((row >> 3) & 1u) << 2);
  if (MODE == 1) return (row & 3u) | (((row >> 4) & 1u) << 2);
  return ((row >> 1) & 1u) | (((row >> 3) & 1u) << 1);
}
template <int RS, int MODE> DEV void stage_chunk(const char* src, unsigned gstride, unsigned lds_base, int w, int lane) {
  constexpr int CPR = RS / 16;
  constexpr int RPI = 64 / CPR;
#pragma unroll
  for (int i = 0; i < 16; ++i) {
    const unsigned piece = (unsigned)(w * 16 + i);
    const unsigned row = piece * RPI + (unsigned)lane / CPR;
    const unsigned ch = ((unsigned)lane % CPR) ^ (rid_of<MODE>(row) << 1);
    glds16(src + (size_t)row * gstride + ch * 16, lds_base + piece * 1024);
  }
}

template <int N> DEV void touch(const uint16_t* p, size_t stride) {
  u32x4 v[N];
#pragma unroll
  for (int i = 0; i < N; ++i) v[i] = *(const u32x4*)(p + i * stride);
#pragma unroll
  for (int i = 0; i < N; ++i) asm volatile("" :: "v"(v[i]));
}

__global__ void __launch_bounds__(256) k_setup(Params p) {
  __shared__ float red[4][16];
  char* wb = ws_base(p.ws);
  const rsrc_t rw = __builtin_amdgcn_make_buffer_rsrc(wb, 0, WS_END, 0x00020000);
  const int bi = blockIdx.x, tid = threadIdx.x, lane = tid & 63, w = tid >> 6;
  const int g = bi & 3, r = bi >> 3, id = r * 4 + g;
  if (bi == 0) STAMP(250, 0);
  const bool hi = (bi & 4) != 0;
  if (r < 32) {
    const unsigned x = xcc_id();
    const size_t co = (x & 3u) * 1024 + (tid & 127) * 8;
    if (x < 4u) touch<PF_A>(((tid >> 7) ? p.wk : p.wq) + (size_t)(16 * r) * 4096 + co, 4096);
    else touch<PF_B>(((tid >> 7) ? p.wk : p.wv) + (size_t)(PF_B * r) * 4096 + co, 4096);
    STAMP(253, r);
  } else if (r >= 32 && r < 56) {
    const int col = ((bi & 7) * 24 + r - 32) * 16 + (tid & 1) * 8, rr = tid >> 1;
    u32x4 v[4];
    float s[4];
#pragma unroll
    for (int i = 0; i < 4; ++i) { v[i] = *(const u32x4*)(p.wmod + (size_t)(i * 128 + rr) * 3072 + col); s[i] = bf2f(p.c[i * 128 + rr]); }
    float acc[8] = {};
#pragma unroll
    for (int i = 0; i < 4; ++i) {
      const float t = rbf(s[i] / (1.f + __expf(-s[i])));
#pragma unroll
      for (int j = 0; j < 4; ++j) { acc[2 * j] += t * bflo(v[i][j]); acc[2 * j + 1] += t * bfhi(v[i][j]); }
    }
#pragma unroll
    for (int j = 0; j < 8; ++j) {
      float t = acc[j];
      t += u2f((unsigned)__builtin_amdgcn_update_dpp(0, (int)f2u(t), 0x122, 0xf, 0xf, true));
      t += u2f((unsigned)__builtin_amdgcn_update_dpp(0, (int)f2u(t), 0x124, 0xf, 0xf, true));
      t += xor8f(t);
      acc[j] = add32(add16(t));
    }
    if (lane < 2) {
#pragma unroll
      for (int j = 0; j < 8; ++j) red[w][lane * 8 + j] = acc[j];
    }
    __syncthreads();
    if (tid < 16) __builtin_amdgcn_raw_buffer_store_b32(f2u(rbf((red[0][tid] + red[1][tid]) + (red[2][tid] + red[3][tid]))), rw, OFF_MOD + (col - (tid & 1) * 8 + tid) * 4, 0, 0);
    STAMP(251, r - 16);
  } else if (!hi) {
    if (r == 56) {
      if (g == 0) st128(u32x4{0, 0, 0, 0}, rw, OFF_CTRL + tid * 16, 0);
#pragma unroll
      for (int i = 0; i < 8; ++i) st128(u32x4{0, 0, 0, 0}, rw, OFF_FLAG + (g * 8 + i) * 4096 + tid * 16, 0);
    }
  } else {
      const int row = (id - 224) * 32 + (tid >> 3);
      const uint16_t* xr = p.x + (size_t)row * 512 + (tid & 7) * 8;
      u32x4 v[8];
#pragma unroll
      for (int k = 0; k < 8; ++k) v[k] = *(const u32x4*)(xr + k * 64);
      float s = 0.f;
#pragma unroll
      for (int k = 0; k < 8; ++k) s += (bflo(v[k].x) + bfhi(v[k].x)) + (bflo(v[k].y) + bfhi(v[k].y)) + (bflo(v[k].z) + bfhi(v[k].z)) + (bflo(v[k].w) + bfhi(v[k].w));
      s += __shfl_xor(s, 1); s += __shfl_xor(s, 2); s += __shfl_xor(s, 4);
      const float mean = s * (1.f / 512.f);
      float q = 0.f;
#pragma unroll
      for (int k = 0; k < 8; ++k) {
#pragma unroll
        for (int j = 0; j < 4; ++j) { const float d0 = bflo(v[k][j]) - mean, d1 = bfhi(v[k][j]) - mean; q += d0 * d0 + d1 * d1; }
      }
      q += __shfl_xor(q, 1); q += __shfl_xor(q, 2); q += __shfl_xor(q, 4);
      const u32x2 st = {f2u(mean), f2u(1.f / sqrtf(q * (1.f / 512.f) + 1e-5f))};
      if ((tid & 7) == 0) __builtin_amdgcn_raw_buffer_store_b64(st, rw, OFF_STAT + row * 8, 0, 0);
  }
}

DEV void role_fill(const Params& p, rsrc_t rw, int j, int tid) {
  const u32x4 ones = {~0u, ~0u, ~0u, ~0u}, z = {0, 0, 0, 0};
#pragma unroll
  for (int i = 0; i < 2; ++i) {
    const int t = tid + 256 * i;
    u32x4 v = ones;
    if (j == 0) {
      const uint16_t* mr = p.mem0 + (2 * ((t >> 3) & 3) + ((t >> 2) & 1)) * 512 + 64 * (t >> 6) + 8 * ((t >> 5) & 1) + 2 * (t & 3);
      v.x = nosent(mr[0] | ((unsigned)mr[16] << 16)); v.y = nosent(mr[32] | ((unsigned)mr[48] << 16));
      v.z = nosent(mr[1] | ((unsigned)mr[17] << 16)); v.w = nosent(mr[33] | ((unsigned)mr[49] << 16));
    }
#pragma unroll
    for (int q = 0; q < 2; ++q) {
      const unsigned o = (unsigned)(j * 16384 + q * 8192 + t * 16);
      st128(v, rw, OFF_COMM + o, 0); st128(ones, rw, OFF_COMM + MB + o, 0);
      st128(z, rw, OFF_TS + o, 0);
    }
  }
  if (tid < 128) st128(z, rw, OFF_CTRL + 524288 + (unsigned)((j >> 3) * 16384 + (tid >> 6) * 8192 + (j & 7) * 1024 + (tid & 63) * 16), 0);
}
DEV void role_vote(rsrc_t rw, int j, int tid) {
  VMWAIT(0);
  BARRIER();
  if (tid < 64) {
    const unsigned o = OFF_CTRL + 524288 + (unsigned)((j >> 3) * 16384 + (j & 7) * 1024);
    unsigned b0 = ~0u, b1 = ~0u;
#pragma unroll 1
    for (int i = 0; i < NVOTE; ++i) {
      unsigned long long t0, t1;
      asm volatile("s_memtime %0\n\ts_waitcnt lgkmcnt(0)" : "=s"(t0) :: "memory");
      unsigned v = ld32(rw, i == 0 ? 0xf0000000u : o + ((i ^ (j >> 2)) & 1) * 8192 + (i >> 1) * 256, 0);
      asm volatile("s_waitcnt vmcnt(0)\n\ts_memtime %0\n\ts_waitcnt lgkmcnt(0)" : "=s"(t1), "+v"(v) :: "memory");
      const unsigned d = i == 0 ? ~0u : (unsigned)(t1 - t0);
      if ((i ^ (j >> 2)) & 1) b1 = min(b1, d); else b0 = min(b0, d);
    }
    if (tid == 0) st32(1u + ((b1 < b0 ? 1u : 0u) ^ (xcc_id() < 4u ? 0u : 1u)), rw, fa0(F_LN1 + j) + 128, 0);
  }
}
DEV Near get_near(rsrc_t rw, int lane) {
  const unsigned vo = fa0(F_LN1 + lane) + 128;
  wait_ge(rw, vo, 1u, false);
  CFENCE();
  Near nr;
  nr.ps = __popcll(__ballot(ld32(rw, vo, 0) == 2u)) > 32 ? 0x5555555555555555ull : 0xaaaaaaaaaaaaaaaaull;
  nr.p0 = nr.ps & 0xfffull;
  nr.fb = OFF_FLAG + (unsigned)((nr.p0 >> 1) & 1ull) * 8192;
  return nr;
}

DEV void role_ln1(const Params& p, rsrc_t rw, int seg, lds_t L, int tid) {
  const int lane = tid & 63, w = tid >> 6, row = lane & 15, g = lane >> 4, tile = w & 1, kh = w >> 1;
  const uint16_t* xr = p.x + (size_t)(seg * 32 + tile * 16 + row) * 512 + kh * 256 + g * 8;
  const u32x4 mv = __builtin_amdgcn_raw_buffer_load_b128(rw, OFF_MOD + tid * 16, 0, 0);
  const u32x2 st = __builtin_amdgcn_raw_buffer_load_b64(rw, OFF_STAT + (seg * 32 + tile * 16 + row) * 8, 0, 0);
  u32x4 v[8];
#pragma unroll
  for (int k = 0; k < 8; ++k) v[k] = *(const u32x4*)(xr + k * 32);
  const float one = tid >= 128 ? 1.f : 0.f;
  *(LAS f32x4*)(L + 1024 + tid * 16) = f32x4{u2f(mv.x) + one, u2f(mv.y) + one, u2f(mv.z) + one, u2f(mv.w) + one};
  BARRIER();
  const float rstd = u2f(st.y), nm = -u2f(st.x) * rstd;
  const lds_t md = L + 1024 + (kh * 256 + g * 8) * 4;
#pragma unroll
  for (int k = 0; k < 8; ++k) {
    const f32x4 s0 = *(LAS f32x4*)(md + k * 128), s1 = *(LAS f32x4*)(md + k * 128 + 16);
    const f32x4 c0 = *(LAS f32x4*)(md + 2048 + k * 128), c1 = *(LAS f32x4*)(md + 2048 + k * 128 + 16);
    u32x4 o;
    o.x = pk2(fmaf(bflo(v[k].x), rstd, nm) * c0.x + s0.x, fmaf(bfhi(v[k].x), rstd, nm) * c0.y + s0.y);
    o.y = pk2(fmaf(bflo(v[k].y), rstd, nm) * c0.z + s0.z, fmaf(bfhi(v[k].y), rstd, nm) * c0.w + s0.w);
    o.z = pk2(fmaf(bflo(v[k].z), rstd, nm) * c1.x + s1.x, fmaf(bfhi(v[k].z), rstd, nm) * c1.y + s1.y);
    o.w = pk2(fmaf(bflo(v[k].w), rstd, nm) * c1.z + s1.z, fmaf(bfhi(v[k].w), rstd, nm) * c1.w + s1.w);
    st128(o, rw, OFF_H1 + (unsigned)(((seg * 2 + tile) * 16 + kh * 8 + k) * 1024 + lane * 16), 0);
  }
  block_publish(rw, fa0(F_LN1 + seg), 1u, tid);
}

DEV void role_qkv(const Params& p, char* wb, rsrc_t rw, int j, lds_t L, int tid) {
  const int lane = tid & 63, w = __builtin_amdgcn_readfirstlane(tid >> 6);
  const int mat = j >> 4, hp = j & 15;
  const uint16_t* W = (mat == 0 ? p.wq : (mat == 1 ? p.wk : p.wv)) + hp * 256;
  const unsigned l0 = (unsigned)(uintptr_t)L;
  u32x32 A[8];
  const int gb = lane >> 4, qq = (lane >> 2) & 3, pp = lane & 3;
  const unsigned rid = (unsigned)(qq | ((gb & 1) << 2));
  stage_chunk<512, 0>((const char*)W, 8192, l0, w, lane);
  stage_chunk<512, 0>((const char*)W + (size_t)128 * 8192, 8192, l0 + 65536, w, lane);
#pragma unroll
  for (int ck = 0; ck < 4; ++ck) {
    if (ck < 3) VMWAIT(16); else VMWAIT(0);
    BARRIER();
#pragma unroll
    for (int t = 0; t < 4; ++t) {
      const unsigned chunk = (unsigned)(w * 8 + (t >> 1) * 4 + pp) ^ (rid << 1);
      const lds_t b = L + (ck & 1) * 65536 + (8 * gb + qq) * 512 + chunk * 16 + (t & 1) * 8;
#pragma unroll
      for (int u = 0; u < 8; ++u) {
        const u32x2 f = ldstr(b + ((u >> 1) * 32 + (u & 1) * 4) * 512);
        A[t * 2 + (ck >> 1)][(ck & 1) * 16 + u * 2] = f.x; A[t * 2 + (ck >> 1)][(ck & 1) * 16 + u * 2 + 1] = f.y;
      }
    }
    BARRIER();
    if (ck < 2) stage_chunk<512, 0>((const char*)W + (size_t)(ck + 2) * 128 * 8192, 8192, l0 + (ck & 1) * 65536, w, lane);
  }
  STAMP(96 + j, 1);
  block_wait(rw, fa0(F_LN1 + lane), 1u, w, false);
  STAMP(96 + j, 2);
  u32x4 cv = {(unsigned)lane * 16, l0 + (unsigned)lane * 16, 0, 0};
  u32x8 cs = {};
  cs[0] = OFF_H1 + (unsigned)w * 8192;
  cs[1] = l0 + (unsigned)w * 8192;
  cs[2] = OFF_QKV + (unsigned)mat * 32 * MB + (unsigned)(hp * 2 + (w >> 1)) * 16384 + (unsigned)(w & 1) * 2048;
  cs[3] = w == 0 ? fa0(mat == 0 ? F_Q + hp : F_KV + j - 16) : 0xf0000000u;
  cs[4] = OFF_DBG + (unsigned)((96 + j) * 256 + 8) * 8;
  asm volatile(
  "s_mov_b32 s61, 0\n\t"
  "s_min_u32 s62, s61, 63\n\t"
  "s_lshl_b32 s62, s62, 15\n\t"
  "s_add_u32 s62, s62, s44\n\t"
  "s_add_u32 s63, s62, 0x1000\n\t"
  "s_and_b32 s61, s61, 3\n\t"
  "s_lshl_b32 s61, s61, 15\n\t"
  "s_add_u32 m0, s61, s45\n\t"
  "s_nop 0\n\t"
  "buffer_load_dwordx4 v0, s[40:43], s62 offen offset:0 sc1 lds\n\t"
  "buffer_load_dwordx4 v0, s[40:43], s62 offen offset:1024 sc1 lds\n\t"
  "buffer_load_dwordx4 v0, s[40:43], s62 offen offset:2048 sc1 lds\n\t"
  "buffer_load_dwordx4 v0, s[40:43], s62 offen offset:3072 sc1 lds\n\t"
  "s_add_u32 m0, m0, 0x1000\n\t"
  "s_nop 0\n\t"
  "buffer_load_dwordx4 v0, s[40:43], s63 offen offset:0 sc1 lds\n\t"
  "buffer_load_dwordx4 v0, s[40:43], s63 offen offset:1024 sc1 lds\n\t"
  "buffer_load_dwordx4 v0, s[40:43], s63 offen offset:2048 sc1 lds\n\t"
  "buffer_load_dwordx4 v0, s[40:43], s63 offen offset:3072 sc1 lds\n\t"
  "s_mov_b32 s61, 1\n\t"
  "s_min_u32 s62, s61, 63\n\t"
  "s_lshl_b32 s62, s62, 15\n\t"
  "s_add_u32 s62, s62, s44\n\t"
  "s_add_u32 s63, s62, 0x1000\n\t"
  "s_and_b32 s61, s61, 3\n\t"
  "s_lshl_b32 s61, s61, 15\n\t"
  "s_add_u32 m0, s61, s45\n\t"
  "s_nop 0\n\t"
  "buffer_load_dwordx4 v0, s[40:43], s62 offen offset:0 sc1 lds\n\t"
  "buffer_load_dwordx4 v0, s[40:43], s62 offen offset:1024 sc1 lds\n\t"
  "buffer_load_dwordx4 v0, s[40:43], s62 offen offset:2048 sc1 lds\n\t"
  "buffer_load_dwordx4 v0, s[40:43], s62 offen offset:3072 sc1 lds\n\t"
  "s_add_u32 m0, m0, 0x1000\n\t"
  "s_nop 0\n\t"
  "buffer_load_dwordx4 v0, s[40:43], s63 offen offset:0 sc1 lds\n\t"
  "buffer_load_dwordx4 v0, s[40:43], s63 offen offset:1024 sc1 lds\n\t"
  "buffer_load_dwordx4 v0, s[40:43], s63 offen offset:2048 sc1 lds\n\t"
  "buffer_load_dwordx4 v0, s[40:43], s63 offen offset:3072 sc1 lds\n\t"
  "s_mov_b32 s60, 0\n\t"
  "s_mov_b32 s65, 0xf0000000\n\t"
  "v_mov_b32 v3, v1\n\t"
  "s_waitcnt vmcnt(8)\n\t"
  "s_barrier\n\t"
  "ds_read_b128 v[40:43], v3 offset:0\n\t"
  "ds_read_b128 v[44:47], v3 offset:1024\n\t"
  "ds_read_b128 v[48:51], v3 offset:2048\n\t"
  ".Lqkv_loop:\n\t"
  "s_waitcnt lgkmcnt(2)\n\t"
  "v_mfma_f32_16x16x32_bf16 v[8:11], a[0:3], v[40:43], 0\n\t"
  "v_mov_b32 v2, v3\n\t"
  "ds_read_b128 v[52:55], v2 offset:3072\n\t"
  "v_mfma_f32_16x16x32_bf16 v[12:15], a[64:67], v[40:43], 0\n\t"
  "s_add_u32 s61, s60, 1\n\t"
  "s_and_b32 s61, s61, 3\n\t"
  "v_mfma_f32_16x16x32_bf16 v[16:19], a[128:131], v[40:43], 0\n\t"
  "s_lshl_b32 s61, s61, 15\n\t"
  "v_add_u32 v3, s61, v1\n\t"
  "v_mfma_f32_16x16x32_bf16 v[20:23], a[192:195], v[40:43], 0\n\t"
  "s_waitcnt lgkmcnt(2)\n\t"
  "v_mfma_f32_16x16x32_bf16 v[8:11], a[4:7], v[44:47], v[8:11]\n\t"
  "ds_read_b128 v[40:43], v2 offset:4096\n\t"
  "v_mfma_f32_16x16x32_bf16 v[12:15], a[68:71], v[44:47], v[12:15]\n\t"
  "v_cvt_pk_bf16_f32 v56, v24, v25\n\t"
  "v_cvt_pk_bf16_f32 v57, v26, v27\n\t"
  "v_mfma_f32_16x16x32_bf16 v[16:19], a[132:135], v[44:47], v[16:19]\n\t"
  "v_cvt_pk_bf16_f32 v58, v28, v29\n\t"
  "v_cvt_pk_bf16_f32 v59, v30, v31\n\t"
  "v_mfma_f32_16x16x32_bf16 v[20:23], a[196:199], v[44:47], v[20:23]\n\t"
  "s_waitcnt lgkmcnt(2)\n\t"
  "v_mfma_f32_16x16x32_bf16 v[8:11], a[8:11], v[48:51], v[8:11]\n\t"
  "ds_read_b128 v[44:47], v2 offset:5120\n\t"
  "v_mfma_f32_16x16x32_bf16 v[12:15], a[72:75], v[48:51], v[12:15]\n\t"
  "v_cvt_pk_bf16_f32 v60, v32, v33\n\t"
  "v_cvt_pk_bf16_f32 v61, v34, v35\n\t"
  "v_mfma_f32_16x16x32_bf16 v[16:19], a[136:139], v[48:51], v[16:19]\n\t"
  "v_cvt_pk_bf16_f32 v62, v36, v37\n\t"
  "v_cvt_pk_bf16_f32 v63, v38, v39\n\t"
  "v_mfma_f32_16x16x32_bf16 v[20:23], a[200:203], v[48:51], v[20:23]\n\t"
  "s_waitcnt lgkmcnt(2)\n\t"
  "buffer_store_dwordx4 v[56:59], v0, s[40:43], s65 offen sc1\n\t"
  "v_mfma_f32_16x16x32_bf16 v[8:11], a[12:15], v[52:55], v[8:11]\n\t"
  "ds_read_b128 v[48:51], v2 offset:6144\n\t"
  "v_mfma_f32_16x16x32_bf16 v[12:15], a[76:79], v[52:55], v[12:15]\n\t"
  "buffer_store_dwordx4 v[60:63], v0, s[40:43], s65 offen offset:1024 sc1\n\t"
  "s_add_u32 s61, s60, 2\n\t"
  "v_mfma_f32_16x16x32_bf16 v[16:19], a[140:143], v[52:55], v[16:19]\n\t"
  "s_min_u32 s62, s61, 63\n\t"
  "s_lshl_b32 s62, s62, 15\n\t"
  "v_mfma_f32_16x16x32_bf16 v[20:23], a[204:207], v[52:55], v[20:23]\n\t"
  "s_waitcnt lgkmcnt(2)\n\t"
  "s_add_u32 s62, s62, s44\n\t"
  "v_mfma_f32_16x16x32_bf16 v[8:11], a[16:19], v[40:43], v[8:11]\n\t"
  "ds_read_b128 v[52:55], v2 offset:7168\n\t"
  "v_mfma_f32_16x16x32_bf16 v[12:15], a[80:83], v[40:43], v[12:15]\n\t"
  "s_add_u32 s63, s62, 0x1000\n\t"
  "s_and_b32 s61, s61, 3\n\t"
  "v_mfma_f32_16x16x32_bf16 v[16:19], a[144:147], v[40:43], v[16:19]\n\t"
  "s_lshl_b32 s61, s61, 15\n\t"
  "s_add_u32 m0, s61, s45\n\t"
  "v_mfma_f32_16x16x32_bf16 v[20:23], a[208:211], v[40:43], v[20:23]\n\t"
  "s_waitcnt lgkmcnt(2)\n\t"
  "v_mfma_f32_16x16x32_bf16 v[8:11], a[20:23], v[44:47], v[8:11]\n\t"
  "ds_read_b128 v[40:43], v2 offset:8192\n\t"
  "v_mfma_f32_16x16x32_bf16 v[12:15], a[84:87], v[44:47], v[12:15]\n\t"
  "buffer_load_dwordx4 v0, s[40:43], s62 offen offset:0 sc1 lds\n\t"
  "v_mfma_f32_16x16x32_bf16 v[16:19], a[148:151], v[44:47], v[16:19]\n\t"
  "v_mfma_f32_16x16x32_bf16 v[20:23], a[212:215], v[44:47], v[20:23]\n\t"
  "s_waitcnt lgkmcnt(2)\n\t"
  "v_mfma_f32_16x16x32_bf16 v[8:11], a[24:27], v[48:51], v[8:11]\n\t"
  "ds_read_b128 v[44:47], v2 offset:9216\n\t"
  "v_mfma_f32_16x16x32_bf16 v[12:15], a[88:91], v[48:51], v[12:15]\n\t"
  "buffer_load_dwordx4 v0, s[40:43], s62 offen offset:1024 sc1 lds\n\t"
  "v_mfma_f32_16x16x32_bf16 v[16:19], a[152:155], v[48:51], v[16:19]\n\t"
  "v_mfma_f32_16x16x32_bf16 v[20:23], a[216:219], v[48:51], v[20:23]\n\t"
  "s_waitcnt lgkmcnt(2)\n\t"
  "v_mfma_f32_16x16x32_bf16 v[8:11], a[28:31], v[52:55], v[8:11]\n\t"
  "ds_read_b128 v[48:51], v2 offset:10240\n\t"
  "v_mfma_f32_16x16x32_bf16 v[12:15], a[92:95], v[52:55], v[12:15]\n\t"
  "buffer_load_dwordx4 v0, s[40:43], s62 offen offset:2048 sc1 lds\n\t"
  "v_mfma_f32_16x16x32_bf16 v[16:19], a[156:159], v[52:55], v[16:19]\n\t"
  "v_mfma_f32_16x16x32_bf16 v[20:23], a[220:223], v[52:55], v[20:23]\n\t"
  "s_waitcnt lgkmcnt(2)\n\t"
  "v_mfma_f32_16x16x32_bf16 v[8:11], a[32:35], v[40:43], v[8:11]\n\t"
  "ds_read_b128 v[52:55], v2 offset:11264\n\t"
  "v_mfma_f32_16x16x32_bf16 v[12:15], a[96:99], v[40:43], v[12:15]\n\t"
  "buffer_load_dwordx4 v0, s[40:43], s62 offen offset:3072 sc1 lds\n\t"
  "v_mfma_f32_16x16x32_bf16 v[16:19], a[160:163], v[40:43], v[16:19]\n\t"
  "v_mfma_f32_16x16x32_bf16 v[20:23], a[224:227], v[40:43], v[20:23]\n\t"
  "s_waitcnt lgkmcnt(2)\n\t"
  "v_mfma_f32_16x16x32_bf16 v[8:11], a[36:39], v[44:47], v[8:11]\n\t"
  "ds_read_b128 v[40:43], v2 offset:12288\n\t"
  "v_mfma_f32_16x16x32_bf16 v[12:15], a[100:103], v[44:47], v[12:15]\n\t"
  "s_add_u32 m0, m0, 0x1000\n\t"
  "s_nop 0\n\t"
  "v_mfma_f32_16x16x32_bf16 v[16:19], a[164:167], v[44:47], v[16:19]\n\t"
  "buffer_load_dwordx4 v0, s[40:43], s63 offen offset:0 sc1 lds\n\t"
  "v_mfma_f32_16x16x32_bf16 v[20:23], a[228:231], v[44:47], v[20:23]\n\t"
  "s_waitcnt lgkmcnt(2)\n\t"
  "v_mfma_f32_16x16x32_bf16 v[8:11], a[40:43], v[48:51], v[8:11]\n\t"
  "ds_read_b128 v[44:47], v2 offset:13312\n\t"
  "v_mfma_f32_16x16x32_bf16 v[12:15], a[104:107], v[48:51], v[12:15]\n\t"
  "buffer_load_dwordx4 v0, s[40:43], s63 offen offset:1024 sc1 lds\n\t"
  "v_mfma_f32_16x16x32_bf16 v[16:19], a[168:171], v[48:51], v[16:19]\n\t"
  "v_mfma_f32_16x16x32_bf16 v[20:23], a[232:235], v[48:51], v[20:23]\n\t"
  "s_waitcnt lgkmcnt(2)\n\t"
  "v_mfma_f32_16x16x32_bf16 v[8:11], a[44:47], v[52:55], v[8:11]\n\t"
  "ds_read_b128 v[48:51], v2 offset:14336\n\t"
  "v_mfma_f32_16x16x32_bf16 v[12:15], a[108:111], v[52:55], v[12:15]\n\t"
  "buffer_load_dwordx4 v0, s[40:43], s63 offen offset:2048 sc1 lds\n\t"
  "v_mfma_f32_16x16x32_bf16 v[16:19], a[172:175], v[52:55], v[16:19]\n\t"
  "v_mfma_f32_16x16x32_bf16 v[20:23], a[236:239], v[52:55], v[20:23]\n\t"
  "s_waitcnt lgkmcnt(2)\n\t"
  "v_mfma_f32_16x16x32_bf16 v[8:11], a[48:51], v[40:43], v[8:11]\n\t"
  "ds_read_b128 v[52:55], v2 offset:15360\n\t"
  "v_mfma_f32_16x16x32_bf16 v[12:15], a[112:115], v[40:43], v[12:15]\n\t"
  "buffer_load_dwordx4 v0, s[40:43], s63 offen offset:3072 sc1 lds\n\t"
  "v_mfma_f32_16x16x32_bf16 v[16:19], a[176:179], v[40:43], v[16:19]\n\t"
  "v_mfma_f32_16x16x32_bf16 v[20:23], a[240:243], v[40:43], v[20:23]\n\t"
  "s_waitcnt lgkmcnt(2)\n\t"
  "v_mfma_f32_16x16x32_bf16 v[8:11], a[52:55], v[44:47], v[8:11]\n\t"
  "ds_read_b128 v[40:43], v2 offset:16384\n\t"
  "v_mfma_f32_16x16x32_bf16 v[12:15], a[116:119], v[44:47], v[12:15]\n\t"
  "v_mfma_f32_16x16x32_bf16 v[16:19], a[180:183], v[44:47], v[16:19]\n\t"
  "v_mfma_f32_16x16x32_bf16 v[20:23], a[244:247], v[44:47], v[20:23]\n\t"
  "s_waitcnt lgkmcnt(2)\n\t"
  "v_mfma_f32_16x16x32_bf16 v[8:11], a[56:59], v[48:51], v[8:11]\n\t"
  "ds_read_b128 v[44:47], v2 offset:17408\n\t"
  "v_mfma_f32_16x16x32_bf16 v[12:15], a[120:123], v[48:51], v[12:15]\n\t"
  "s_lshr_b32 s61, s46, 21\n\t"
  "s_bitcmp1_b64 s[50:51], s61\n\t"
  "v_mfma_f32_16x16x32_bf16 v[16:19], a[184:187], v[48:51], v[16:19]\n\t"
  "s_cselect_b32 s61, 0x2000, 0\n\t"
  "s_add_u32 s64, s46, s61\n\t"
  "v_mfma_f32_16x16x32_bf16 v[20:23], a[248:251], v[48:51], v[20:23]\n\t"
  "s_waitcnt lgkmcnt(2)\n\t"
  "v_mfma_f32_16x16x32_bf16 v[8:11], a[60:63], v[52:55], v[8:11]\n\t"
  "ds_read_b128 v[48:51], v2 offset:18432\n\t"
  "v_mfma_f32_16x16x32_bf16 v[12:15], a[124:127], v[52:55], v[12:15]\n\t"
  "s_waitcnt vmcnt(8)\n\t"
  "s_barrier\n\t"
  "v_mfma_f32_16x16x32_bf16 v[16:19], a[188:191], v[52:55], v[16:19]\n\t"
  "v_mfma_f32_16x16x32_bf16 v[20:23], a[252:255], v[52:55], v[20:23]\n\t"
  "s_waitcnt lgkmcnt(2)\n\t"
  "v_mfma_f32_16x16x32_bf16 v[24:27], a[0:3], v[40:43], 0\n\t"
  "ds_read_b128 v[52:55], v2 offset:19456\n\t"
  "v_mfma_f32_16x16x32_bf16 v[28:31], a[64:67], v[40:43], 0\n\t"
  "v_mov_b32 v4, s60\n\t"
  "v_mfma_f32_16x16x32_bf16 v[32:35], a[128:131], v[40:43], 0\n\t"
  "s_mov_b64 exec, 1\n\t"
  "buffer_store_dword v4, off, s[40:43], s47 sc1\n\t"
  "s_mov_b64 exec, -1\n\t"
  "v_mfma_f32_16x16x32_bf16 v[36:39], a[192:195], v[40:43], 0\n\t"
  "s_waitcnt lgkmcnt(2)\n\t"
  "v_mfma_f32_16x16x32_bf16 v[24:27], a[4:7], v[44:47], v[24:27]\n\t"
  "ds_read_b128 v[40:43], v2 offset:20480\n\t"
  "v_mfma_f32_16x16x32_bf16 v[28:31], a[68:71], v[44:47], v[28:31]\n\t"
  "v_cvt_pk_bf16_f32 v56, v8, v9\n\t"
  "v_cvt_pk_bf16_f32 v57, v10, v11\n\t"
  "v_mfma_f32_16x16x32_bf16 v[32:35], a[132:135], v[44:47], v[32:35]\n\t"
  "v_cvt_pk_bf16_f32 v58, v12, v13\n\t"
  "v_cvt_pk_bf16_f32 v59, v14, v15\n\t"
  "v_mfma_f32_16x16x32_bf16 v[36:39], a[196:199], v[44:47], v[36:39]\n\t"
  "s_waitcnt lgkmcnt(2)\n\t"
  "v_mfma_f32_16x16x32_bf16 v[24:27], a[8:11], v[48:51], v[24:27]\n\t"
  "ds_read_b128 v[44:47], v2 offset:21504\n\t"
  "v_mfma_f32_16x16x32_bf16 v[28:31], a[72:75], v[48:51], v[28:31]\n\t"
  "v_cvt_pk_bf16_f32 v60, v16, v17\n\t"
  "v_cvt_pk_bf16_f32 v61, v18, v19\n\t"
  "v_mfma_f32_16x16x32_bf16 v[32:35], a[136:139], v[48:51], v[32:35]\n\t"
  "v_cvt_pk_bf16_f32 v62, v20, v21\n\t"
  "v_cvt_pk_bf16_f32 v63, v22, v23\n\t"
  "v_mfma_f32_16x16x32_bf16 v[36:39], a[200:203], v[48:51], v[36:39]\n\t"
  "s_waitcnt lgkmcnt(2)\n\t"
  "buffer_store_dwordx4 v[56:59], v0, s[40:43], s64 offen sc1\n\t"
  "v_mfma_f32_16x16x32_bf16 v[24:27], a[12:15], v[52:55], v[24:27]\n\t"
  "ds_read_b128 v[48:51], v2 offset:22528\n\t"
  "v_mfma_f32_16x16x32_bf16 v[28:31], a[76:79], v[52:55], v[28:31]\n\t"
  "buffer_store_dwordx4 v[60:63], v0, s[40:43], s64 offen offset:1024 sc1\n\t"
  "s_add_u32 s65, s64, 0x1000\n\t"
  "v_mfma_f32_16x16x32_bf16 v[32:35], a[140:143], v[52:55], v[32:35]\n\t"
  "s_add_u32 s46, s46, 0x80000\n\t"
  "s_add_u32 s60, s60, 1\n\t"
  "v_mfma_f32_16x16x32_bf16 v[36:39], a[204:207], v[52:55], v[36:39]\n\t"
  "s_waitcnt lgkmcnt(2)\n\t"
  "v_mfma_f32_16x16x32_bf16 v[24:27], a[16:19], v[40:43], v[24:27]\n\t"
  "ds_read_b128 v[52:55], v2 offset:23552\n\t"
  "v_mfma_f32_16x16x32_bf16 v[28:31], a[80:83], v[40:43], v[28:31]\n\t"
  "v_mfma_f32_16x16x32_bf16 v[32:35], a[144:147], v[40:43], v[32:35]\n\t"
  "v_mfma_f32_16x16x32_bf16 v[36:39], a[208:211], v[40:43], v[36:39]\n\t"
  "s_waitcnt lgkmcnt(2)\n\t"
  "v_mfma_f32_16x16x32_bf16 v[24:27], a[20:23], v[44:47], v[24:27]\n\t"
  "ds_read_b128 v[40:43], v2 offset:24576\n\t"
  "v_mfma_f32_16x16x32_bf16 v[28:31], a[84:87], v[44:47], v[28:31]\n\t"
  "v_mfma_f32_16x16x32_bf16 v[32:35], a[148:151], v[44:47], v[32:35]\n\t"
  "v_mfma_f32_16x16x32_bf16 v[36:39], a[212:215], v[44:47], v[36:39]\n\t"
  "s_waitcnt lgkmcnt(2)\n\t"
  "v_mfma_f32_16x16x32_bf16 v[24:27], a[24:27], v[48:51], v[24:27]\n\t"
  "ds_read_b128 v[44:47], v2 offset:25600\n\t"
  "v_mfma_f32_16x16x32_bf16 v[28:31], a[88:91], v[48:51], v[28:31]\n\t"
  "v_mfma_f32_16x16x32_bf16 v[32:35], a[152:155], v[48:51], v[32:35]\n\t"
  "v_mfma_f32_16x16x32_bf16 v[36:39], a[216:219], v[48:51], v[36:39]\n\t"
  "s_waitcnt lgkmcnt(2)\n\t"
  "v_mfma_f32_16x16x32_bf16 v[24:27], a[28:31], v[52:55], v[24:27]\n\t"
  "ds_read_b128 v[48:51], v2 offset:26624\n\t"
  "v_mfma_f32_16x16x32_bf16 v[28:31], a[92:95], v[52:55], v[28:31]\n\t"
  "v_mfma_f32_16x16x32_bf16 v[32:35], a[156:159], v[52:55], v[32:35]\n\t"
  "v_mfma_f32_16x16x32_bf16 v[36:39], a[220:223], v[52:55], v[36:39]\n\t"
  "s_waitcnt lgkmcnt(2)\n\t"
  "v_mfma_f32_16x16x32_bf16 v[24:27], a[32:35], v[40:43], v[24:27]\n\t"
  "ds_read_b128 v[52:55], v2 offset:27648\n\t"
  "v_mfma_f32_16x16x32_bf16 v[28:31], a[96:99], v[40:43], v[28:31]\n\t"
  "v_mfma_f32_16x16x32_bf16 v[32:35], a[160:163], v[40:43], v[32:35]\n\t"
  "v_mfma_f32_16x16x32_bf16 v[36:39], a[224:227], v[40:43], v[36:39]\n\t"
  "s_waitcnt lgkmcnt(2)\n\t"
  "v_mfma_f32_16x16x32_bf16 v[24:27], a[36:39], v[44:47], v[24:27]\n\t"
  "ds_read_b128 v[40:43], v2 offset:28672\n\t"
  "v_mfma_f32_16x16x32_bf16 v[28:31], a[100:103], v[44:47], v[28:31]\n\t"
  "v_mfma_f32_16x16x32_bf16 v[32:35], a[164:167], v[44:47], v[32:35]\n\t"
  "v_mfma_f32_16x16x32_bf16 v[36:39], a[228:231], v[44:47], v[36:39]\n\t"
  "s_waitcnt lgkmcnt(2)\n\t"
  "v_mfma_f32_16x16x32_bf16 v[24:27], a[40:43], v[48:51], v[24:27]\n\t"
  "ds_read_b128 v[44:47], v2 offset:29696\n\t"
  "v_mfma_f32_16x16x32_bf16 v[28:31], a[104:107], v[48:51], v[28:31]\n\t"
  "v_mfma_f32_16x16x32_bf16 v[32:35], a[168:171], v[48:51], v[32:35]\n\t"
  "v_mfma_f32_16x16x32_bf16 v[36:39], a[232:235], v[48:51], v[36:39]\n\t"
  "s_waitcnt lgkmcnt(2)\n\t"
  "v_mfma_f32_16x16x32_bf16 v[24:27], a[44:47], v[52:55], v[24:27]\n\t"
  "ds_read_b128 v[48:51], v2 offset:30720\n\t"
  "v_mfma_f32_16x16x32_bf16 v[28:31], a[108:111], v[52:55], v[28:31]\n\t"
  "v_mfma_f32_16x16x32_bf16 v[32:35], a[172:175], v[52:55], v[32:35]\n\t"
  "v_mfma_f32_16x16x32_bf16 v[36:39], a[236:239], v[52:55], v[36:39]\n\t"
  "s_waitcnt lgkmcnt(2)\n\t"
  "v_mfma_f32_16x16x32_bf16 v[24:27], a[48:51], v[40:43], v[24:27]\n\t"
  "ds_read_b128 v[52:55], v2 offset:31744\n\t"
  "v_mfma_f32_16x16x32_bf16 v[28:31], a[112:115], v[40:43], v[28:31]\n\t"
  "v_mfma_f32_16x16x32_bf16 v[32:35], a[176:179], v[40:43], v[32:35]\n\t"
  "v_mfma_f32_16x16x32_bf16 v[36:39], a[240:243], v[40:43], v[36:39]\n\t"
  "s_waitcnt lgkmcnt(2)\n\t"
  "v_mfma_f32_16x16x32_bf16 v[24:27], a[52:55], v[44:47], v[24:27]\n\t"
  "ds_read_b128 v[40:43], v3 offset:0\n\t"
  "v_mfma_f32_16x16x32_bf16 v[28:31], a[116:119], v[44:47], v[28:31]\n\t"
  "v_mfma_f32_16x16x32_bf16 v[32:35], a[180:183], v[44:47], v[32:35]\n\t"
  "v_mfma_f32_16x16x32_bf16 v[36:39], a[244:247], v[44:47], v[36:39]\n\t"
  "s_waitcnt lgkmcnt(2)\n\t"
  "v_mfma_f32_16x16x32_bf16 v[24:27], a[56:59], v[48:51], v[24:27]\n\t"
  "ds_read_b128 v[44:47], v3 offset:1024\n\t"
  "v_mfma_f32_16x16x32_bf16 v[28:31], a[120:123], v[48:51], v[28:31]\n\t"
  "s_cmp_lt_u32 s60, 64\n\t"
  "v_mfma_f32_16x16x32_bf16 v[32:35], a[184:187], v[48:51], v[32:35]\n\t"
  "v_mfma_f32_16x16x32_bf16 v[36:39], a[248:251], v[48:51], v[36:39]\n\t"
  "s_waitcnt lgkmcnt(2)\n\t"
  "v_mfma_f32_16x16x32_bf16 v[24:27], a[60:63], v[52:55], v[24:27]\n\t"
  "ds_read_b128 v[48:51], v3 offset:2048\n\t"
  "v_mfma_f32_16x16x32_bf16 v[28:31], a[124:127], v[52:55], v[28:31]\n\t"
  "v_mfma_f32_16x16x32_bf16 v[32:35], a[188:191], v[52:55], v[32:35]\n\t"
  "v_mfma_f32_16x16x32_bf16 v[36:39], a[252:255], v[52:55], v[36:39]\n\t"
  "s_waitcnt lgkmcnt(2)\n\t"
  "s_cbranch_scc1 .Lqkv_loop\n\t"
  "v_cvt_pk_bf16_f32 v56, v24, v25\n\t"
  "v_cvt_pk_bf16_f32 v57, v26, v27\n\t"
  "v_cvt_pk_bf16_f32 v58, v28, v29\n\t"
  "v_cvt_pk_bf16_f32 v59, v30, v31\n\t"
  "v_cvt_pk_bf16_f32 v60, v32, v33\n\t"
  "v_cvt_pk_bf16_f32 v61, v34, v35\n\t"
  "v_cvt_pk_bf16_f32 v62, v36, v37\n\t"
  "v_cvt_pk_bf16_f32 v63, v38, v39\n\t"
  "buffer_store_dwordx4 v[56:59], v0, s[40:43], s65 offen sc1\n\t"
  "buffer_store_dwordx4 v[60:63], v0, s[40:43], s65 offen offset:1024 sc1\n\t"
  "s_waitcnt vmcnt(0) lgkmcnt(0)\n\t"
  "s_barrier\n\t"
  "v_mov_b32 v4, 64\n\t"
  "s_mov_b64 exec, 1\n\t"
  "buffer_store_dword v4, off, s[40:43], s47 sc1\n\t"
  "s_mov_b64 exec, -1\n\t"
  "s_waitcnt vmcnt(0)\n\t"
    : "+{v[0:3]}"(cv), "+{s[44:51]}"(cs)
    : "{a[0:31]}"(A[0]), "{a[32:63]}"(A[1]), "{a[64:95]}"(A[2]), "{a[96:127]}"(A[3]), "{a[128:159]}"(A[4]), "{a[160:191]}"(A[5]), "{a[192:223]}"(A[6]), "{a[224:255]}"(A[7]),
      "{s[40:43]}"(rw)
    : "memory", "vcc", "scc", "v4", "v5", "v6", "v7", "v8", "v9", "v10", "v11", "v12", "v13", "v14", "v15", "v16", "v17", "v18", "v19", "v20", "v21", "v22", "v23", "v24", "v25", "v26", "v27", "v28", "v29", "v30", "v31", "v32", "v33", "v34", "v35", "v36", "v37", "v38", "v39", "v40", "v41", "v42", "v43", "v44", "v45", "v46", "v47", "v48", "v49", "v50", "v51", "v52", "v53", "v54", "v55", "v56", "v57", "v58", "v59", "v60", "v61", "v62", "v63", "v64", "v65", "v66", "v67", "v68", "v69", "v70", "v71", "v72", "v73", "v74", "v75", "v76", "v77", "v78", "v79", "s60", "s61", "s62", "s63", "s64", "s65", "s66", "s67", "s68", "s69", "s70", "s71");
  __builtin_amdgcn_endpgm();
}

DEV void role_rec(const Params& p, char* wb, rsrc_t rw, int c, lds_t L, int tid) {
  const int lane = tid & 63, w = __builtin_amdgcn_readfirstlane(tid >> 6);
  const int h = c >> 1, a = c & 1;
  const unsigned l0 = (unsigned)(uintptr_t)L;
  const int gb = lane >> 4, qq = (lane >> 2) & 3, pp = lane & 3, n16 = lane & 15;
  constexpr int L_EV = 8448, L_WO = 16384, L_KV = 81920, WO_W = 16384;
  u32x32 W[12];
  const char* src[3] = {(const char*)(p.wq + h * 128), (const char*)(p.wk + h * 128), (const char*)(p.wv + h * 128)};
  const char* wo = (const char*)(p.wo + (size_t)h * 128 * 512 + a * 256);
  constexpr int LOC[48] = {0, 4, 9, 11, 43, 44, 15, 17, 40, 42, 10, 12, 13, 14, 16, 18, 41, 7, 28, 30, 32, 34, 36, 38, 3, 8, 29, 31, 33, 35, 37, 39, 1, 5, 19, 21, 22, 46, 25, 26, 2, 6, 20, 45, 23, 24, 47, 27};
  const unsigned rot = (unsigned)(w ^ (h & 3));
  stage_chunk<256, 1>(src[0] + (size_t)a * 256 * 8192, 8192, l0 + L_KV, w, lane);
  stage_chunk<256, 1>(src[0] + (size_t)(1 - a) * 256 * 8192, 8192, l0 + L_WO, w, lane);
  const unsigned rowl = (unsigned)(16 * (qq & 1) + 2 * (qq >> 1) + 4 * (gb >> 1) + (gb & 1));
  const unsigned rid = rid_of<1>(rowl);
#pragma unroll
  for (int ck = 0; ck < 6; ++ck) {
    VMWAIT(16);
    BARRIER();
#pragma unroll
    for (int t = 0; t < 2; ++t) {
      const unsigned chunk = (unsigned)(4 * w + 2 * t + (pp >> 1)) ^ (rid << 1);
      const lds_t b = L + ((ck & 1) ? L_WO : L_KV) + rowl * 256 + chunk * 16 + (pp & 1) * 8;
#pragma unroll
      for (int T = 0; T < 4; ++T)
#pragma unroll
        for (int u = 0; u < 4; ++u) {
          const u32x2 f = ldstr(b + ((T ^ rot) * 16384u) + (32 * (u & 1) + 8 * (u >> 1)) * 256);
          const int lc = LOC[(ck >> 1) * 16 + t * 8 + 2 * T + (ck & 1)];
          W[lc >> 2][(lc & 3) * 8 + u * 2] = f.x; W[lc >> 2][(lc & 3) * 8 + u * 2 + 1] = f.y;
        }
    }
    BARRIER();
    if (ck + 2 < 6) stage_chunk<256, 1>(src[(ck + 2) >> 1] + (size_t)((ck & 1) ^ a) * 256 * 8192, 8192, l0 + ((ck & 1) ? L_WO : L_KV), w, lane);
    else if (ck == 4) stage_chunk<512, 0>(wo, 1024, l0 + L_KV, w, lane);
  }
#pragma unroll
  for (int T = 0; T < 8; ++T) {
    const u32x32 f = W[10 + (T >> 2)];
    const int o = (T & 3) * 8;
    *(LAS u32x4*)(L + L_WO + w * WO_W + T * 2048 + lane * 16) = u32x4{f[o], f[o + 1], f[o + 2], f[o + 3]};
    *(LAS u32x4*)(L + L_WO + w * WO_W + T * 2048 + 1024 + lane * 16) = u32x4{f[o + 4], f[o + 5], f[o + 6], f[o + 7]};
  }
#pragma unroll
  for (int i = 0; i < 3; ++i) *(LAS u32x4*)(L + i * 4096 + tid * 16) = u32x4{0, 0, 0, 0};
  VMWAIT(0);
  BARRIER();
  {
    const unsigned rid0 = (unsigned)(qq | ((gb & 1) << 2));
#pragma unroll
    for (int t = 0; t < 4; ++t) {
      const unsigned chunk = (unsigned)(8 * w + 2 * t + (pp >> 1)) ^ (rid0 << 1);
      const lds_t b = L + L_KV + (8 * gb + qq) * 512 + chunk * 16 + (pp & 1) * 8;
#pragma unroll
      for (int u = 0; u < 8; ++u) {
        const u32x2 f = ldstr(b + ((u >> 1) * 32 + (u & 1) * 4) * 512);
        W[10 + (t >> 1)][(t & 1) * 16 + u * 2] = f.x; W[10 + (t >> 1)][(t & 1) * 16 + u * 2 + 1] = f.y;
      }
    }
  }
  BARRIER();
  const Near nr = get_near(rw, lane);
  const unsigned par2 = ~(unsigned)nr.p0, par = par2 & 1u;
  const unsigned cm = OFF_COMM + par * 8192;
  const int prow = 8 * ((n16 >> 2) & 1) + 4 * ((n16 >> 1) & 1) + 2 * (n16 >> 3) + (n16 & 1);
  const unsigned rho = (unsigned)((4 * a + w) * 8 + (n16 >> 3) * 4 + gb);
  u32x16 cv = {}, cs = {};
  cv[0] = (unsigned)lane * 16;
  cv[1] = ((lane & 2) ? 0xEEEEu : 0x4444u) | ((lane & 4) ? 0xEEEE0000u : 0x44440000u);
  cv[2] = (unsigned)((gb >> 1) * 512 + (n16 >> 2) * 128 + (pp & 1) * 64 + (gb & 1) * 32 + (pp >> 1) * 16);
  cv[3] = l0 + (unsigned)(((gb & 1) * 260 + (gb >> 1) * 528 + 64 * w + n16) * 8);
  cv[4] = l0 + L_EV + (unsigned)(((gb & 1) * 136 + 32 * w + n16) * 8 + (gb >> 1) * 4);
  cv[5] = l0 + (unsigned)(((pp & 1) * 260 + (pp >> 1) * 528 + 8 * gb + qq) * 8);
  cv[6] = l0 + L_EV + (unsigned)(((pp >> 1) * 136 + 16 * gb + 4 * (qq >> 1) + (qq & 1) + 2 * (pp & 1)) * 8);
  cv[7] = l0 + L_KV + (unsigned)((gb * 16 + prow) * 16);
  cv[8] = l0 + L_WO + (unsigned)(w * WO_W + lane * 16);
  cv[9] = cm + (64 + (rho >> 1)) * 16384 + (rho & 1) * 4096 + (unsigned)h * 128 + (unsigned)(lane & 7) * 16;
  cs[0] = cm + rot * 1024 + (unsigned)a * 4096;
  cs[1] = OFF_QKV + (w == 3 ? 64 * MB + 4096 : 32 * MB + (unsigned)(w - 1) * 4096) + (unsigned)h * 16384;
  cs[2] = l0 + L_KV + (w == 3 ? 12288u : (unsigned)(w - 1) * 4096);
  cs[3] = cm + (64 + ((unsigned)c >> 1)) * 16384 + ((unsigned)c & 1) * 4096;
  cs[4] = OFF_KVM + hm((unsigned)(h * 4096 + a * 2048 + w * 512));
  cs[5] = (unsigned)w;
  cs[6] = w == 0 ? fa(nr, F_REC + c) : 0xf0000000u;
  cs[7] = fa0(F_KV + (h >> 1));
  cs[8] = (unsigned)c * 128;
  cs[9] = l0 + (a ? L_EV + (unsigned)w * 256 : 256 + (unsigned)w * 512);
  cs[10] = OFF_CTRL + C_ERR;
  cs[11] = l0;
  cs[12] = f2u(0.08838834764831845f * 1.4426950408889634f);
  cs[13] = a ? 136 * 8 : 260 * 8;
  cs[14] = w == 0 ? OFF_TS + (par2 & 2u) * 4096 : 0xf0000000u;
  cs[15] = a ? 4 : 528 * 8;
  STAMP(c, 1);
  asm volatile(
  "s_mov_b32 s28, s58\n\t"
  "s_mov_b32 s30, s59\n\t"
  "s_mov_b32 s60, 0\n\t"
  "s_mov_b32 s58, 0xcccccccc\n\t"
  "s_mov_b32 s59, 0xcccccccc\n\t"
  "s_mov_b32 s70, 0x3f803f80\n\t"
  "s_mov_b32 s71, 0x3e803e80\n\t"
  "s_mov_b32 s39, 0\n\t"
  "s_and_b32 s29, s44, 0xffffe000\n\t"
  "s_add_u32 s29, s29, s52\n\t"
  "s_add_u32 s29, s29, 0x4000\n\t"
  "s_mov_b32 s75, 0xf0000000\n\t"
  "s_xor_b32 s61, s44, 0x1000\n\t"
  "s_xor_b32 s67, s49, 0\n\t"
  "s_lshl_b32 s67, s67, 11\n\t"
  "s_add_u32 s67, s67, s55\n\t"
  "s_add_u32 s34, s67, 147456\n\t"
  "s_xor_b32 s67, s49, 1\n\t"
  "s_lshl_b32 s67, s67, 11\n\t"
  "s_add_u32 s67, s67, s55\n\t"
  "s_add_u32 s35, s67, 147456\n\t"
  "s_xor_b32 s67, s49, 2\n\t"
  "s_lshl_b32 s67, s67, 11\n\t"
  "s_add_u32 s67, s67, s55\n\t"
  "s_add_u32 s36, s67, 147456\n\t"
  "s_xor_b32 s67, s49, 3\n\t"
  "s_lshl_b32 s67, s67, 11\n\t"
  "s_add_u32 s67, s67, s55\n\t"
  "s_add_u32 s37, s67, 147456\n\t"
  "v_mov_b32 v3, 0\n\t"
  "v_mov_b32 v7, 0\n\t"
  "v_mov_b32 v11, 0\n\t"
  "v_mov_b32 v15, 0\n\t"
  "v_mov_b32 v19, 0\n\t"
  "v_mov_b32 v23, 0\n\t"
  "v_mov_b32 v27, 0\n\t"
  "v_mov_b32 v31, 0\n\t"
  "s_mov_b32 s66, 0\n\t"
  ".Lrec_kva:\n\t"
  "v_and_b32 v22, 16, v32\n\t"
  "v_lshlrev_b32 v22, 8, v22\n\t"
  "buffer_load_dword v26, v22, s[40:43], s51 offen sc1\n\t"
  "s_waitcnt vmcnt(0)\n\t"
  "v_cmp_gt_u32 vcc, 1, v26\n\t"
  "s_cbranch_vccz .Lrec_kvoka\n\t"
  "s_sleep 1\n\t"
  "s_add_u32 s66, s66, 1\n\t"
  "s_and_b32 s67, s66, 255\n\t"
  "s_cmp_lg_u32 s67, 0\n\t"
  "s_cbranch_scc1 .Lrec_kva\n\t"
  "buffer_load_dword v18, off, s[40:43], s54 sc1\n\t"
  "s_waitcnt vmcnt(0)\n\t"
  "v_readfirstlane_b32 s67, v18\n\t"
  "s_nop 1\n\t"
  "s_cmp_lg_u32 s67, 0\n\t"
  "s_cbranch_scc1 .Lrec_end\n\t"
  "s_cmp_lt_u32 s66, 0x80000\n\t"
  "s_cbranch_scc1 .Lrec_kva\n\t"
  "v_mov_b32 v18, 1\n\t"
  "buffer_store_dword v18, off, s[40:43], s54 sc1\n\t"
  "s_branch .Lrec_end\n\t"
  ".Lrec_kvoka:\n\t"
  "s_sub_u32 s67, s49, 1\n\t"
  "s_lshl_b32 s67, s67, 11\n\t"
  "s_sub_u32 s31, s45, s67\n\t"
  "s_add_u32 s31, s31, 33554432\n\t"
  "s_sub_u32 s38, s46, s67\n\t"
  "s_add_u32 s38, s38, 8192\n\t"
  "s_cmp_eq_u32 s49, 0\n\t"
  "s_cbranch_scc1 .Lrec_p0\n\t"
  "s_mov_b32 s21, 0\n\t"
  "s_and_b32 s68, s21, 3\n\t"
  "s_lshl_b32 s68, s68, 14\n\t"
  "s_add_u32 m0, s46, s68\n\t"
  "s_min_u32 s67, s21, 63\n\t"
  "s_lshl_b32 s67, s67, 19\n\t"
  "s_add_u32 s62, s45, s67\n\t"
  "s_lshr_b32 s78, s62, 21\n\t"
  "s_bitcmp1_b64 s[76:77], s78\n\t"
  "s_cselect_b32 s78, 0x2000, 0\n\t"
  "s_add_u32 s62, s62, s78\n\t"
  "buffer_load_dwordx4 v32, s[40:43], s62 offen offset:0 sc1 lds\n\t"
  "buffer_load_dwordx4 v32, s[40:43], s62 offen offset:1024 sc1 lds\n\t"
  "buffer_load_dwordx4 v32, s[40:43], s62 offen offset:2048 sc1 lds\n\t"
  "buffer_load_dwordx4 v32, s[40:43], s62 offen offset:3072 sc1 lds\n\t"
  "s_cmp_eq_u32 s49, 3\n\t"
  "s_cbranch_scc1 .Lrec_nda\n\t"
  "s_add_u32 m0, s38, s68\n\t"
  "s_add_u32 s62, s31, s67\n\t"
  "s_lshr_b32 s78, s62, 21\n\t"
  "s_bitcmp1_b64 s[76:77], s78\n\t"
  "s_cselect_b32 s78, 0x2000, 0\n\t"
  "s_add_u32 s62, s62, s78\n\t"
  "buffer_load_dwordx4 v32, s[40:43], s62 offen offset:0 sc1 lds\n\t"
  "buffer_load_dwordx4 v32, s[40:43], s62 offen offset:1024 sc1 lds\n\t"
  ".Lrec_nda:\n\t"
  ".Lrec_p0:\n\t"
  "s_waitcnt vmcnt(0)\n\t"
  "s_barrier\n\t"
  "s_add_u32 s69, s55, 90112\n\t"
  "v_add_u32 v43, s69, v32\n\t"
  "ds_read_b128 v[44:47], v43 offset:0\n\t"
  "ds_read_b128 v[48:51], v43 offset:1024\n\t"
  "ds_read_b128 v[52:55], v43 offset:2048\n\t"
  "ds_read_b128 v[56:59], v43 offset:3072\n\t"
  "ds_read_b128 v[60:63], v43 offset:4096\n\t"
  "ds_read_b128 v[64:67], v43 offset:5120\n\t"
  "ds_read_b128 v[68:71], v43 offset:6144\n\t"
  "ds_read_b128 v[72:75], v43 offset:7168\n\t"
  "s_waitcnt lgkmcnt(7)\n\t"
  "v_mfma_f32_16x16x32_bf16 v[76:79], v[44:47], v[192:195], 0\n\t"
  "s_waitcnt lgkmcnt(6)\n\t"
  "v_mfma_f32_16x16x32_bf16 v[76:79], v[48:51], v[196:199], v[76:79]\n\t"
  "s_waitcnt lgkmcnt(5)\n\t"
  "v_mfma_f32_16x16x32_bf16 v[76:79], v[52:55], v[200:203], v[76:79]\n\t"
  "s_waitcnt lgkmcnt(4)\n\t"
  "v_mfma_f32_16x16x32_bf16 v[76:79], v[56:59], v[204:207], v[76:79]\n\t"
  "s_waitcnt lgkmcnt(3)\n\t"
  "v_mfma_f32_16x16x32_bf16 v[80:83], v[60:63], v[192:195], 0\n\t"
  "s_waitcnt lgkmcnt(2)\n\t"
  "v_mfma_f32_16x16x32_bf16 v[80:83], v[64:67], v[196:199], v[80:83]\n\t"
  "s_waitcnt lgkmcnt(1)\n\t"
  "v_mfma_f32_16x16x32_bf16 v[80:83], v[68:71], v[200:203], v[80:83]\n\t"
  "v_cvt_pk_bf16_f32 v0, v76, v77\n\t"
  "v_cvt_pk_bf16_f32 v4, v78, v79\n\t"
  "s_waitcnt lgkmcnt(0)\n\t"
  "v_mfma_f32_16x16x32_bf16 v[80:83], v[72:75], v[204:207], v[80:83]\n\t"
  "v_mfma_f32_16x16x32_bf16 v[76:79], v[44:47], v[208:211], 0\n\t"
  "v_mfma_f32_16x16x32_bf16 v[76:79], v[48:51], v[212:215], v[76:79]\n\t"
  "v_mfma_f32_16x16x32_bf16 v[76:79], v[52:55], v[216:219], v[76:79]\n\t"
  "v_cvt_pk_bf16_f32 v1, v80, v81\n\t"
  "v_cvt_pk_bf16_f32 v5, v82, v83\n\t"
  "v_mfma_f32_16x16x32_bf16 v[76:79], v[56:59], v[220:223], v[76:79]\n\t"
  "v_mfma_f32_16x16x32_bf16 v[80:83], v[60:63], v[208:211], 0\n\t"
  "v_mfma_f32_16x16x32_bf16 v[80:83], v[64:67], v[212:215], v[80:83]\n\t"
  "v_mfma_f32_16x16x32_bf16 v[80:83], v[68:71], v[216:219], v[80:83]\n\t"
  "v_cvt_pk_bf16_f32 v8, v76, v77\n\t"
  "v_cvt_pk_bf16_f32 v12, v78, v79\n\t"
  "v_mfma_f32_16x16x32_bf16 v[80:83], v[72:75], v[220:223], v[80:83]\n\t"
  "v_mfma_f32_16x16x32_bf16 v[76:79], v[44:47], v[224:227], 0\n\t"
  "v_mfma_f32_16x16x32_bf16 v[76:79], v[48:51], v[228:231], v[76:79]\n\t"
  "v_mfma_f32_16x16x32_bf16 v[76:79], v[52:55], v[232:235], v[76:79]\n\t"
  "v_cvt_pk_bf16_f32 v9, v80, v81\n\t"
  "v_cvt_pk_bf16_f32 v13, v82, v83\n\t"
  "v_mfma_f32_16x16x32_bf16 v[76:79], v[56:59], v[236:239], v[76:79]\n\t"
  "v_mfma_f32_16x16x32_bf16 v[80:83], v[60:63], v[224:227], 0\n\t"
  "v_mfma_f32_16x16x32_bf16 v[80:83], v[64:67], v[228:231], v[80:83]\n\t"
  "v_mfma_f32_16x16x32_bf16 v[80:83], v[68:71], v[232:235], v[80:83]\n\t"
  "v_cvt_pk_bf16_f32 v16, v76, v77\n\t"
  "v_cvt_pk_bf16_f32 v20, v78, v79\n\t"
  "v_mfma_f32_16x16x32_bf16 v[80:83], v[72:75], v[236:239], v[80:83]\n\t"
  "v_mfma_f32_16x16x32_bf16 v[76:79], v[44:47], v[240:243], 0\n\t"
  "v_mfma_f32_16x16x32_bf16 v[76:79], v[48:51], v[244:247], v[76:79]\n\t"
  "v_mfma_f32_16x16x32_bf16 v[76:79], v[52:55], v[248:251], v[76:79]\n\t"
  "v_cvt_pk_bf16_f32 v17, v80, v81\n\t"
  "v_cvt_pk_bf16_f32 v21, v82, v83\n\t"
  "v_mfma_f32_16x16x32_bf16 v[76:79], v[56:59], v[252:255], v[76:79]\n\t"
  "v_mfma_f32_16x16x32_bf16 v[80:83], v[60:63], v[240:243], 0\n\t"
  "v_mfma_f32_16x16x32_bf16 v[80:83], v[64:67], v[244:247], v[80:83]\n\t"
  "v_mfma_f32_16x16x32_bf16 v[80:83], v[68:71], v[248:251], v[80:83]\n\t"
  "v_cvt_pk_bf16_f32 v24, v76, v77\n\t"
  "v_cvt_pk_bf16_f32 v28, v78, v79\n\t"
  "v_mfma_f32_16x16x32_bf16 v[80:83], v[72:75], v[252:255], v[80:83]\n\t"
  "s_nop 7\n\t"
  "v_cvt_pk_bf16_f32 v25, v80, v81\n\t"
  "v_cvt_pk_bf16_f32 v29, v82, v83\n\t"
  "s_mov_b32 s66, 0\n\t"
  ".Lrec_kvb:\n\t"
  "v_and_b32 v22, 16, v32\n\t"
  "v_lshlrev_b32 v22, 8, v22\n\t"
  "buffer_load_dword v26, v22, s[40:43], s51 offen sc1\n\t"
  "s_waitcnt vmcnt(0)\n\t"
  "v_cmp_gt_u32 vcc, 2, v26\n\t"
  "s_cbranch_vccz .Lrec_kvokb\n\t"
  "s_sleep 1\n\t"
  "s_add_u32 s66, s66, 1\n\t"
  "s_and_b32 s67, s66, 255\n\t"
  "s_cmp_lg_u32 s67, 0\n\t"
  "s_cbranch_scc1 .Lrec_kvb\n\t"
  "buffer_load_dword v18, off, s[40:43], s54 sc1\n\t"
  "s_waitcnt vmcnt(0)\n\t"
  "v_readfirstlane_b32 s67, v18\n\t"
  "s_nop 1\n\t"
  "s_cmp_lg_u32 s67, 0\n\t"
  "s_cbranch_scc1 .Lrec_end\n\t"
  "s_cmp_lt_u32 s66, 0x80000\n\t"
  "s_cbranch_scc1 .Lrec_kvb\n\t"
  "v_mov_b32 v18, 1\n\t"
  "buffer_store_dword v18, off, s[40:43], s54 sc1\n\t"
  "s_branch .Lrec_end\n\t"
  ".Lrec_kvokb:\n\t"
  "s_cmp_eq_u32 s49, 0\n\t"
  "s_cbranch_scc1 .Lrec_top\n\t"
  "s_mov_b32 s21, 1\n\t"
  "s_and_b32 s68, s21, 3\n\t"
  "s_lshl_b32 s68, s68, 14\n\t"
  "s_add_u32 m0, s46, s68\n\t"
  "s_min_u32 s67, s21, 63\n\t"
  "s_lshl_b32 s67, s67, 19\n\t"
  "s_add_u32 s62, s45, s67\n\t"
  "s_lshr_b32 s78, s62, 21\n\t"
  "s_bitcmp1_b64 s[76:77], s78\n\t"
  "s_cselect_b32 s78, 0x2000, 0\n\t"
  "s_add_u32 s62, s62, s78\n\t"
  "buffer_load_dwordx4 v32, s[40:43], s62 offen offset:0 sc1 lds\n\t"
  "buffer_load_dwordx4 v32, s[40:43], s62 offen offset:1024 sc1 lds\n\t"
  "buffer_load_dwordx4 v32, s[40:43], s62 offen offset:2048 sc1 lds\n\t"
  "buffer_load_dwordx4 v32, s[40:43], s62 offen offset:3072 sc1 lds\n\t"
  "s_cmp_eq_u32 s49, 3\n\t"
  "s_cbranch_scc1 .Lrec_ndb\n\t"
  "s_add_u32 m0, s38, s68\n\t"
  "s_add_u32 s62, s31, s67\n\t"
  "s_lshr_b32 s78, s62, 21\n\t"
  "s_bitcmp1_b64 s[76:77], s78\n\t"
  "s_cselect_b32 s78, 0x2000, 0\n\t"
  "s_add_u32 s62, s62, s78\n\t"
  "buffer_load_dwordx4 v32, s[40:43], s62 offen offset:0 sc1 lds\n\t"
  "buffer_load_dwordx4 v32, s[40:43], s62 offen offset:1024 sc1 lds\n\t"
  ".Lrec_ndb:\n\t"
  ".Lrec_top:\n\t"
  "buffer_load_dwordx4 v[96:99], v34, s[40:43], s44 offen sc1\n\t"
  "buffer_load_dwordx4 v[100:103], v34, s[40:43], s61 offen sc1\n\t"
  "v_lshrrev_b32 v70, 2, v32\n\t"
  "v_bfe_u32 v71, v32, 4, 1\n\t"
  "v_and_b32 v70, 0xf8, v70\n\t"
  "v_mad_u32_u24 v70, v71, s57, v70\n\t"
  "v_add_u32 v70, s53, v70\n\t"
  "v_add_u32 v71, s30, v70\n\t"
  "ds_read_b32 v68, v70\n\t"
  "ds_read_b32 v69, v71\n\t"
  "v_lshrrev_b32 v70, 1, v32\n\t"
  "ds_read_b128 v[44:47], v40 offset:0\n\t"
  "ds_read_b128 v[48:51], v40 offset:1024\n\t"
  "v_mov_b64 v[72:73], 0\n\t"
  "v_mov_b64 v[74:75], 0\n\t"
  "v_mov_b64 v[76:77], 0\n\t"
  "v_mov_b64 v[78:79], 0\n\t"
  "ds_read_b128 v[52:55], v40 offset:2048\n\t"
  "ds_read_b128 v[56:59], v40 offset:3072\n\t"
  "v_mov_b64 v[80:81], 0\n\t"
  "v_mov_b64 v[82:83], 0\n\t"
  "v_mov_b64 v[84:85], 0\n\t"
  "v_mov_b64 v[86:87], 0\n\t"
  "ds_read_b128 v[60:63], v40 offset:4096\n\t"
  "ds_read_b128 v[64:67], v40 offset:5120\n\t"
  "v_mov_b64 v[88:89], 0\n\t"
  "v_mov_b64 v[90:91], 0\n\t"
  "v_mov_b64 v[92:93], 0\n\t"
  "v_mov_b64 v[94:95], 0\n\t"
  "s_and_b32 s67, s60, 1\n\t"
  "s_lshl_b32 s65, s67, 19\n\t"
  "s_add_u32 s64, s47, s65\n\t"
  "v_mov_b64 v[104:105], -1\n\t"
  "v_mov_b64 v[106:107], -1\n\t"
  "s_xor_b32 s63, s65, 0x80000\n\t"
  "s_add_u32 s63, s63, s47\n\t"
  "s_lshl_b32 s67, s49, 10\n\t"
  "s_add_u32 s63, s63, s67\n\t"
  "s_waitcnt lgkmcnt(6)\n\t"
  "buffer_store_dwordx2 v[68:69], v70, s[40:43], s75 offen sc1\n\t"
  "buffer_store_dwordx4 v[104:107], v32, s[40:43], s63 offen sc1\n\t"
  "s_add_u32 s67, s60, 1\n\t"
  "s_and_b32 s67, s67, 3\n\t"
  "s_lshl_b32 s67, s67, 14\n\t"
  "s_add_u32 s69, s67, s55\n\t"
  "s_add_u32 s69, s69, 90112\n\t"
  "s_and_b32 s67, s60, 3\n\t"
  "s_lshl_b32 s67, s67, 14\n\t"
  "v_add_u32 v43, s67, v39\n\t"
  "s_add_u32 s74, s28, s52\n\t"
  "v_add_u32 v2, s34, v32\n\t"
  "v_add_u32 v6, s35, v32\n\t"
  "v_add_u32 v10, s36, v32\n\t"
  "v_add_u32 v14, s37, v32\n\t"
  "ds_read_b128 v[68:71], v43 offset:3072\n\t"
  "s_waitcnt vmcnt(3) lgkmcnt(0)\n\t"
  ".Lrec_mgo:\n\t"
  "ds_write_b128 v2, v[96:99]\n\t"
  "v_smfmac_f32_16x16x64_bf16 v[72:75], v[96:99], a[0:7], v33\n\t"
  "s_waitcnt vmcnt(2)\n\t"
  "ds_write_b128 v2, v[100:103] offset:1024\n\t"
  "v_max3_u32 v42, v96, v97, v98\n\t"
  "v_smfmac_f32_16x16x64_bf16 v[76:79], v[96:99], v[44:51], v33\n\t"
  "v_max3_u32 v42, v42, v99, v100\n\t"
  "v_smfmac_f32_16x16x64_bf16 v[80:83], v[96:99], a[8:15], v33\n\t"
  "ds_read_b128 v[44:47], v40 offset:6144\n\t"
  "v_max3_u32 v42, v42, v101, v102\n\t"
  "v_smfmac_f32_16x16x64_bf16 v[84:87], v[96:99], a[16:23], v33\n\t"
  "ds_read_b128 v[48:51], v40 offset:7168\n\t"
  "v_max_u32 v42, v42, v103\n\t"
  "v_smfmac_f32_16x16x64_bf16 v[88:91], v[96:99], v[52:59], v33\n\t"
  "v_cmp_eq_u32 vcc, -1, v42\n\t"
  "v_smfmac_f32_16x16x64_bf16 v[92:95], v[96:99], a[24:31], v33\n\t"
  "ds_read_b128 v[52:55], v40 offset:8192\n\t"
  "v_smfmac_f32_16x16x64_bf16 v[72:75], v[100:103], a[32:39], v33\n\t"
  "s_cbranch_vccnz .Lrec_mre\n\t"
  "v_smfmac_f32_16x16x64_bf16 v[76:79], v[100:103], v[60:67], v33\n\t"
  "s_waitcnt lgkmcnt(3)\n\t"
  "s_barrier\n\t"
  "v_smfmac_f32_16x16x64_bf16 v[80:83], v[100:103], a[40:47], v33\n\t"
  "ds_read_b128 v[104:107], v6 offset:0\n\t"
  "v_smfmac_f32_16x16x64_bf16 v[84:87], v[100:103], a[48:55], v33\n\t"
  "ds_read_b128 v[108:111], v6 offset:1024\n\t"
  "v_smfmac_f32_16x16x64_bf16 v[88:91], v[100:103], a[56:63], v33\n\t"
  "ds_read_b128 v[112:115], v10 offset:0\n\t"
  "v_smfmac_f32_16x16x64_bf16 v[92:95], v[100:103], a[64:71], v33\n\t"
  "s_waitcnt lgkmcnt(2)\n\t"
  "ds_read_b128 v[116:119], v10 offset:1024\n\t"
  "v_smfmac_f32_16x16x64_bf16 v[72:75], v[104:107], a[72:79], v33\n\t"
  "ds_read_b128 v[120:123], v14 offset:0\n\t"
  "v_smfmac_f32_16x16x64_bf16 v[76:79], v[104:107], a[80:87], v33\n\t"
  "s_waitcnt lgkmcnt(3)\n\t"
  "ds_read_b128 v[124:127], v14 offset:1024\n\t"
  "v_smfmac_f32_16x16x64_bf16 v[72:75], v[108:111], a[88:95], v33\n\t"
  "ds_read_b128 v[56:59], v40 offset:9216\n\t"
  "v_smfmac_f32_16x16x64_bf16 v[76:79], v[108:111], a[96:103], v33\n\t"
  "s_waitcnt lgkmcnt(4)\n\t"
  "ds_read_b128 v[60:63], v40 offset:10240\n\t"
  "v_smfmac_f32_16x16x64_bf16 v[72:75], v[112:115], v[44:51], v33\n\t"
  "ds_read_b128 v[64:67], v40 offset:11264\n\t"
  "v_smfmac_f32_16x16x64_bf16 v[76:79], v[112:115], a[104:111], v33\n\t"
  "s_waitcnt lgkmcnt(2)\n\t"
  "s_waitcnt lgkmcnt(2)\n\t"
  "ds_read_b128 v[44:47], v40 offset:12288\n\t"
  "v_smfmac_f32_16x16x64_bf16 v[72:75], v[116:119], v[52:59], v33\n\t"
  "ds_read_b128 v[48:51], v40 offset:13312\n\t"
  "s_memrealtime s[8:9]\n\t"
  "v_smfmac_f32_16x16x64_bf16 v[76:79], v[116:119], a[112:119], v33\n\t"
  "ds_read_b128 v[52:55], v40 offset:14336\n\t"
  "s_memrealtime s[10:11]\n\t"
  "v_smfmac_f32_16x16x64_bf16 v[72:75], v[120:123], a[120:127], v33\n\t"
  "ds_read_b128 v[56:59], v40 offset:15360\n\t"
  "s_memrealtime s[12:13]\n\t"
  "v_smfmac_f32_16x16x64_bf16 v[76:79], v[120:123], a[128:135], v33\n\t"
  "s_memrealtime s[14:15]\n\t"
  "v_smfmac_f32_16x16x64_bf16 v[72:75], v[124:127], a[136:143], v33\n\t"
  "s_memrealtime s[16:17]\n\t"
  "v_smfmac_f32_16x16x64_bf16 v[76:79], v[124:127], a[144:151], v33\n\t"
  "s_memrealtime s[18:19]\n\t"
  "s_memtime s[22:23]\n\t"
  "v_smfmac_f32_16x16x64_bf16 v[80:83], v[104:107], a[152:159], v33\n\t"
  "v_smfmac_f32_16x16x64_bf16 v[84:87], v[104:107], a[160:167], v33\n\t"
  "v_add_f32 v72, v72, v74\n\t"
  "v_smfmac_f32_16x16x64_bf16 v[80:83], v[108:111], a[168:175], v33\n\t"
  "s_waitcnt lgkmcnt(4)\n\t"
  "s_waitcnt lgkmcnt(0)\n\t"
  "s_add_u32 s8, s8, s10\n\t"
  "v_add_f32 v73, v73, v75\n\t"
  "v_smfmac_f32_16x16x64_bf16 v[84:87], v[108:111], v[60:67], v33\n\t"
  "s_add_u32 s12, s12, s14\n\t"
  "s_add_u32 s16, s16, s18\n\t"
  "v_cvt_pk_bf16_f32 v72, v72, v73\n\t"
  "v_smfmac_f32_16x16x64_bf16 v[80:83], v[112:115], a[176:183], v33\n\t"
  "ds_write2_b32 v35, v72, v72 offset0:0 offset1:1\n\t"
  "v_add_f32 v76, v76, v78\n\t"
  "v_smfmac_f32_16x16x64_bf16 v[84:87], v[112:115], a[184:191], v33\n\t"
  "ds_read_b128 v[60:63], v43 offset:2048\n\t"
  "v_add_f32 v77, v77, v79\n\t"
  "v_smfmac_f32_16x16x64_bf16 v[80:83], v[116:119], v[44:51], v33\n\t"
  "s_add_u32 s20, s8, s12\n\t"
  "s_add_u32 s20, s20, s16\n\t"
  "v_cvt_pk_bf16_f32 v76, v76, v77\n\t"
  "v_smfmac_f32_16x16x64_bf16 v[84:87], v[116:119], a[192:199], v33\n\t"
  "ds_write2_b32 v35, v76, v76 offset0:32 offset1:33\n\t"
  "v_or_b32_e64 v30, s20, 1\n\t"
  "v_smfmac_f32_16x16x64_bf16 v[80:83], v[120:123], a[200:207], v33\n\t"
  "s_mov_b64 exec, 1\n\t"
  "buffer_store_dword v30, off, s[40:43], s74 sc1\n\t"
  "s_mov_b64 exec, -1\n\t"
  "v_smfmac_f32_16x16x64_bf16 v[84:87], v[120:123], v[52:59], v33\n\t"
  "ds_read_b128 v[64:67], v43 offset:6144\n\t"
  "v_mov_b32 v26, s60\n\t"
  "v_smfmac_f32_16x16x64_bf16 v[80:83], v[124:127], a[208:215], v33\n\t"
  "ds_read_b128 v[44:47], v43 offset:0\n\t"
  "v_smfmac_f32_16x16x64_bf16 v[84:87], v[124:127], a[216:223], v33\n\t"
  "ds_read_b128 v[48:51], v43 offset:4096\n\t"
  "v_smfmac_f32_16x16x64_bf16 v[88:91], v[104:107], a[224:231], v33\n\t"
  "s_waitcnt lgkmcnt(3)\n\t"
  "s_barrier\n\t"
  "v_smfmac_f32_16x16x64_bf16 v[92:95], v[104:107], a[232:239], v33\n\t"
  "ds_read_b64_tr_b16 v[76:77], v37 offset:0\n\t"
  "v_add_f32 v80, v80, v82\n\t"
  "v_smfmac_f32_16x16x64_bf16 v[88:91], v[108:111], a[240:247], v33\n\t"
  "ds_read_b64_tr_b16 v[78:79], v37 offset:32\n\t"
  "v_add_f32 v81, v81, v83\n\t"
  "v_smfmac_f32_16x16x64_bf16 v[92:95], v[108:111], a[248:255], v33\n\t"
  "ds_read_b128 v[52:55], v43 offset:1024\n\t"
  "v_cvt_pk_bf16_f32 v80, v80, v81\n\t"
  "v_smfmac_f32_16x16x64_bf16 v[88:91], v[112:115], v[128:135], v33\n\t"
  "ds_read_b128 v[56:59], v43 offset:5120\n\t"
  "v_add_f32 v84, v84, v86\n\t"
  "v_smfmac_f32_16x16x64_bf16 v[92:95], v[112:115], v[136:143], v33\n\t"
  "ds_read_b64_tr_b16 v[96:97], v37 offset:512\n\t"
  "v_add_f32 v85, v85, v87\n\t"
  "v_smfmac_f32_16x16x64_bf16 v[88:91], v[116:119], v[144:151], v33\n\t"
  "ds_read_b64_tr_b16 v[98:99], v37 offset:544\n\t"
  "v_cvt_pk_bf16_f32 v84, v84, v85\n\t"
  "v_smfmac_f32_16x16x64_bf16 v[92:95], v[116:119], v[152:159], v33\n\t"
  "ds_write2_b32 v36, v80, v84 offset0:0 offset1:32\n\t"
  "v_smfmac_f32_16x16x64_bf16 v[88:91], v[120:123], v[160:167], v33\n\t"
  "ds_read_b64_tr_b16 v[100:101], v37 offset:1024\n\t"
  "v_mov_b64 v[116:117], 0\n\t"
  "v_smfmac_f32_16x16x64_bf16 v[92:95], v[120:123], v[168:175], v33\n\t"
  "ds_read_b64_tr_b16 v[102:103], v37 offset:1056\n\t"
  "v_mov_b64 v[118:119], 0\n\t"
  "v_smfmac_f32_16x16x64_bf16 v[88:91], v[124:127], v[176:183], v33\n\t"
  "ds_read_b64_tr_b16 v[104:105], v37 offset:1536\n\t"
  "v_mov_b64 v[120:121], 0\n\t"
  "v_smfmac_f32_16x16x64_bf16 v[92:95], v[124:127], v[184:191], v33\n\t"
  "s_waitcnt lgkmcnt(8)\n\t"
  "ds_read_b64_tr_b16 v[106:107], v37 offset:1568\n\t"
  "v_mov_b64 v[122:123], 0\n\t"
  "v_mfma_f32_16x16x32_bf16 v[108:111], v[44:47], v[76:79], 0\n\t"
  "s_waitcnt lgkmcnt(4)\n\t"
  "s_barrier\n\t"
  "ds_read_b128 v[72:75], v43 offset:7168\n\t"
  "v_mfma_f32_16x16x32_bf16 v[112:115], v[48:51], v[76:79], 0\n\t"
  "s_cmp_eq_u32 s60, 63\n\t"
  "v_add_f32 v88, v88, v90\n\t"
  "v_add_f32 v89, v89, v91\n\t"
  "v_mfma_f32_16x16x32_bf16 v[108:111], v[52:55], v[96:99], v[108:111]\n\t"
  "ds_read_b64_tr_b16 v[80:81], v38 offset:0\n\t"
  "v_cvt_pk_bf16_f32 v88, v88, v89\n\t"
  "v_add_f32 v92, v92, v94\n\t"
  "v_mfma_f32_16x16x32_bf16 v[112:115], v[56:59], v[96:99], v[112:115]\n\t"
  "s_waitcnt lgkmcnt(4)\n\t"
  "ds_read_b64_tr_b16 v[82:83], v38 offset:64\n\t"
  "v_add_f32 v93, v93, v95\n\t"
  "v_cvt_pk_bf16_f32 v92, v92, v93\n\t"
  "v_mfma_f32_16x16x32_bf16 v[108:111], v[60:63], v[100:103], v[108:111]\n\t"
  "ds_write2_b32 v35, v88, v92 offset0:64 offset1:96\n\t"
  "v_mfma_f32_16x16x32_bf16 v[112:115], v[64:67], v[100:103], v[112:115]\n\t"
  "s_waitcnt lgkmcnt(4)\n\t"
  "ds_read_b64_tr_b16 v[84:85], v38 offset:512\n\t"
  "v_mov_b64 v[124:125], 0\n\t"
  "v_mfma_f32_16x16x32_bf16 v[108:111], v[68:71], v[104:107], v[108:111]\n\t"
  "s_waitcnt lgkmcnt(4)\n\t"
  "ds_read_b64_tr_b16 v[86:87], v38 offset:576\n\t"
  "v_mov_b64 v[126:127], 0\n\t"
  "v_mfma_f32_16x16x32_bf16 v[112:115], v[72:75], v[104:107], v[112:115]\n\t"
  "s_waitcnt lgkmcnt(3)\n\t"
  "s_cbranch_scc1 .Lrec_last\n\t"
  "v_smfmac_f32_16x16x64_bf16 v[116:119], v[80:83], v[192:199], v33 abid:1\n\t"
  "s_waitcnt vmcnt(1)\n\t"
  "v_mov_b64 v[88:89], 0\n\t"
  "v_smfmac_f32_16x16x64_bf16 v[120:123], v[80:83], v[208:215], v33 abid:1\n\t"
  "s_waitcnt lgkmcnt(0)\n\t"
  "s_barrier\n\t"
  "v_smfmac_f32_16x16x64_bf16 v[116:119], v[84:87], v[200:207], v33 abid:1\n\t"
  "ds_read_b64_tr_b16 v[44:45], v37 offset:256\n\t"
  "ds_read_b64_tr_b16 v[46:47], v37 offset:288\n\t"
  "v_cndmask_b32 v108, v108, v112, s[58:59]\n\t"
  "v_smfmac_f32_16x16x64_bf16 v[120:123], v[84:87], v[216:223], v33 abid:1\n\t"
  "ds_read_b64_tr_b16 v[48:49], v37 offset:768\n\t"
  "ds_read_b64_tr_b16 v[50:51], v37 offset:800\n\t"
  "v_cndmask_b32 v109, v109, v113, s[58:59]\n\t"
  "v_smfmac_f32_16x16x64_bf16 v[124:127], v[80:83], v[224:231], v33 abid:1\n\t"
  "ds_read_b64_tr_b16 v[52:53], v37 offset:1280\n\t"
  "ds_read_b64_tr_b16 v[54:55], v37 offset:1312\n\t"
  "v_cndmask_b32 v110, v110, v114, s[58:59]\n\t"
  "v_smfmac_f32_16x16x64_bf16 v[124:127], v[84:87], v[232:239], v33 abid:1\n\t"
  "s_waitcnt lgkmcnt(4)\n\t"
  "ds_read_b64_tr_b16 v[56:57], v37 offset:1792\n\t"
  "ds_read_b64_tr_b16 v[58:59], v37 offset:1824\n\t"
  "v_cndmask_b32 v111, v111, v115, s[58:59]\n\t"
  "v_mfma_f32_16x16x32_bf16 v[112:115], v[44:47], v[76:79], 0\n\t"
  "s_waitcnt lgkmcnt(4)\n\t"
  "v_max3_f32 v42, v108, v109, v110\n\t"
  "v_mov_b64 v[90:91], 0\n\t"
  "v_mfma_f32_16x16x32_bf16 v[112:115], v[48:51], v[96:99], v[112:115]\n\t"
  "s_waitcnt lgkmcnt(2)\n\t"
  "v_max_f32 v42, v42, v111\n\t"
  "v_lshlrev_b32 v44, 3, v32\n\t"
  "v_mfma_f32_16x16x32_bf16 v[112:115], v[52:55], v[100:103], v[112:115]\n\t"
  "s_waitcnt lgkmcnt(0)\n\t"
  "buffer_load_dword v96, v44, s[40:43], s28 offen sc1\n\t"
  "v_max_f32_dpp v42, v42, v42 quad_perm:[2,3,0,1] row_mask:0xf bank_mask:0xf\n\t"
  "v_mfma_f32_16x16x32_bf16 v[112:115], v[56:59], v[104:107], v[112:115]\n\t"
  "v_cvt_pk_bf16_f32 v2, v116, v117\n\t"
  "v_cvt_pk_bf16_f32 v6, v118, v119\n\t"
  "v_smfmac_f32_16x16x64_bf16 v[88:91], v[80:83], v[240:247], v33 abid:1\n\t"
  "s_mov_b64 exec, 1\n\t"
  "buffer_store_dword v26, off, s[40:43], s50 sc1\n\t"
  "s_mov_b64 exec, -1\n\t"
  "v_smfmac_f32_16x16x64_bf16 v[88:91], v[84:87], v[248:255], v33 abid:1\n\t"
  "s_nop 0\n\t"
  "v_max3_f32 v42, v42, v112, v113\n\t"
  "v_mov_b32 v43, v42\n\t"
  "v_mov_b64 v[92:93], 0\n\t"
  "v_mov_b64 v[94:95], 0\n\t"
  "v_permlane16_swap_b32 v42, v43\n\t"
  "v_max_f32 v42, v42, v43\n\t"
  "v_mov_b32 v43, v42\n\t"
  "v_mov_b64 v[100:101], 0\n\t"
  "v_mov_b64 v[102:103], 0\n\t"
  "v_permlane32_swap_b32 v42, v43\n\t"
  "v_max_f32 v42, v42, v43\n\t"
  "v_mul_f32 v42, s56, v42\n\t"
  "v_fma_f32 v112, v112, s56, -v42\n\t"
  "v_fma_f32 v113, v113, s56, -v42\n\t"
  "v_fma_f32 v108, v108, s56, -v42\n\t"
  "v_fma_f32 v109, v109, s56, -v42\n\t"
  "v_fma_f32 v110, v110, s56, -v42\n\t"
  "v_fma_f32 v111, v111, s56, -v42\n\t"
  "v_exp_f32 v112, v112\n\t"
  "v_exp_f32 v113, v113\n\t"
  "v_exp_f32 v108, v108\n\t"
  "v_exp_f32 v109, v109\n\t"
  "v_exp_f32 v110, v110\n\t"
  "v_exp_f32 v111, v111\n\t"
  "v_cvt_pk_bf16_f32 v115, v112, v113\n\t"
  "v_cvt_pk_bf16_f32 v113, v112, v113\n\t"
  "v_cvt_pk_bf16_f32 v112, v108, v109\n\t"
  "v_cvt_pk_bf16_f32 v114, v110, v111\n\t"
  "v_mov_b64 v[80:81], s[70:71]\n\t"
  "v_mov_b64 v[82:83], s[70:71]\n\t"
  "v_smfmac_f32_16x16x64_bf16 v[92:95], v[112:115], v[0:7], v33\n\t"
  "v_cvt_pk_bf16_f32 v10, v120, v121\n\t"
  "v_mfma_f32_16x16x32_bf16 v[84:87], v[112:115], v[80:83], 0\n\t"
  "v_cvt_pk_bf16_f32 v14, v122, v123\n\t"
  "v_add_u32 v43, s69, v32\n\t"
  "v_cvt_pk_bf16_f32 v18, v124, v125\n\t"
  "v_smfmac_f32_16x16x64_bf16 v[100:103], v[112:115], v[8:15], v33\n\t"
  "ds_read_b128 v[44:47], v43 offset:0\n\t"
  "v_cvt_pk_bf16_f32 v22, v126, v127\n\t"
  "v_mov_b64 v[104:105], 0\n\t"
  "v_mov_b64 v[106:107], 0\n\t"
  "v_cvt_pk_bf16_f32 v26, v88, v89\n\t"
  "v_cvt_pk_bf16_f32 v30, v90, v91\n\t"
  "v_smfmac_f32_16x16x64_bf16 v[104:107], v[112:115], v[16:23], v33\n\t"
  "ds_read_b128 v[48:51], v43 offset:1024\n\t"
  "v_mov_b64 v[76:77], 0\n\t"
  "v_mov_b64 v[78:79], 0\n\t"
  "v_add_f32 v84, v84, v86\n\t"
  "v_add_f32 v85, v85, v87\n\t"
  "v_smfmac_f32_16x16x64_bf16 v[76:79], v[112:115], v[24:31], v33\n\t"
  "ds_read_b128 v[52:55], v43 offset:2048\n\t"
  "v_rcp_f32 v84, v84\n\t"
  "v_rcp_f32 v85, v85\n\t"
  "v_pk_add_f32 v[92:93], v[92:93], v[94:95]\n\t"
  "v_pk_add_f32 v[100:101], v[100:101], v[102:103]\n\t"
  "v_pk_add_f32 v[104:105], v[104:105], v[106:107]\n\t"
  "s_nop 1\n\t"
  "v_pk_add_f32 v[76:77], v[76:77], v[78:79]\n\t"
  "v_pk_mul_f32 v[92:93], v[92:93], v[84:85]\n\t"
  "v_pk_mul_f32 v[100:101], v[100:101], v[84:85]\n\t"
  "v_pk_mul_f32 v[104:105], v[104:105], v[84:85]\n\t"
  "v_pk_mul_f32 v[76:77], v[76:77], v[84:85]\n\t"
  "v_cvt_pk_bf16_f32 v88, v92, v93\n\t"
  "v_cvt_pk_bf16_f32 v89, v100, v101\n\t"
  "v_cvt_pk_bf16_f32 v90, v104, v105\n\t"
  "v_cvt_pk_bf16_f32 v91, v76, v77\n\t"
  "buffer_store_dwordx4 v[88:91], v41, s[40:43], s65 offen sc1\n\t"
  ".Lrec_pst:\n\t"
  "s_cmp_lg_u32 s49, 0\n\t"
  "s_cbranch_scc1 .Lrec_oth\n\t"
  "s_mov_b32 s66, 0\n\t"
  "s_waitcnt vmcnt(2) lgkmcnt(0)\n\t"
  ".Lrec_ts:\n\t"
  "s_memtime s[24:25]\n\t"
  "v_readfirstlane_b32 s26, v96\n\t"
  "s_mov_b32 s27, 0\n\t"
  "s_sub_u32 s26, s26, 200\n\t"
  "v_subrev_u32 v97, s26, v96\n\t"
  "v_cmp_lt_u32 vcc, 400, v97\n\t"
  "s_cbranch_vccnz .Lrec_tbad\n\t"
  ".Lrec_tok:\n\t"
  "s_waitcnt lgkmcnt(0)\n\t"
  "s_sub_u32 s67, s24, s22\n\t"
  "s_sub_u32 s67, s67, s27\n\t"
  "s_sub_u32 s67, s67, 1400\n\t"
  "s_ashr_i32 s67, s67, 2\n\t"
  "s_add_u32 s67, s67, s20\n\t"
  "s_mov_b32 s66, 0\n\t"
  ".Lrec_ula:\n\t"
  "v_cmp_lt_u32 vcc, s67, v96\n\t"
  "s_add_u32 s67, s67, 9\n\t"
  "s_cbranch_vccz .Lrec_uga\n\t"
  "v_cmp_lt_u32 vcc, s67, v96\n\t"
  "s_add_u32 s67, s67, 9\n\t"
  "s_cbranch_vccz .Lrec_uga\n\t"
  "v_cmp_lt_u32 vcc, s67, v96\n\t"
  "s_add_u32 s67, s67, 9\n\t"
  "s_cbranch_vccz .Lrec_uga\n\t"
  "v_cmp_lt_u32 vcc, s67, v96\n\t"
  "s_add_u32 s67, s67, 9\n\t"
  "s_cbranch_vccz .Lrec_uga\n\t"
  "v_cmp_lt_u32 vcc, s67, v96\n\t"
  "s_add_u32 s67, s67, 9\n\t"
  "s_cbranch_vccz .Lrec_uga\n\t"
  "v_cmp_lt_u32 vcc, s67, v96\n\t"
  "s_add_u32 s67, s67, 9\n\t"
  "s_cbranch_vccz .Lrec_uga\n\t"
  "v_cmp_lt_u32 vcc, s67, v96\n\t"
  "s_add_u32 s67, s67, 9\n\t"
  "s_cbranch_vccz .Lrec_uga\n\t"
  "v_cmp_lt_u32 vcc, s67, v96\n\t"
  "s_add_u32 s67, s67, 9\n\t"
  "s_cbranch_vccz .Lrec_uga\n\t"
  "v_cmp_lt_u32 vcc, s67, v96\n\t"
  "s_add_u32 s67, s67, 9\n\t"
  "s_cbranch_vccz .Lrec_uga\n\t"
  "v_cmp_lt_u32 vcc, s67, v96\n\t"
  "s_add_u32 s67, s67, 9\n\t"
  "s_cbranch_vccz .Lrec_uga\n\t"
  "s_add_u32 s67, s67, 9\n\t"
  "s_add_u32 s66, s66, 1\n\t"
  "s_cmp_lt_u32 s66, 30\n\t"
  "s_cbranch_scc1 .Lrec_ula\n\t"
  ".Lrec_uga:\n\t"
  "buffer_load_dwordx4 v[100:103], v32, s[40:43], s64 offen offset:0 sc1\n\t"
  "buffer_load_dwordx4 v[104:107], v32, s[40:43], s64 offen offset:1024 sc1\n\t"
  "buffer_load_dwordx4 v[108:111], v32, s[40:43], s64 offen offset:2048 sc1\n\t"
  "buffer_load_dwordx4 v[112:115], v32, s[40:43], s64 offen offset:3072 sc1\n\t"
  "s_barrier\n\t"
  "ds_read_b128 v[56:59], v43 offset:3072\n\t"
  "ds_read_b128 v[60:63], v43 offset:4096\n\t"
  "ds_read_b128 v[64:67], v43 offset:5120\n\t"
  "ds_read_b128 v[68:71], v43 offset:6144\n\t"
  "ds_read_b128 v[72:75], v43 offset:7168\n\t"
  "v_mfma_f32_16x16x32_bf16 v[76:79], v[44:47], v[192:195], 0\n\t"
  "v_max3_u32 v42, v88, v89, v90\n\t"
  "v_max_u32 v42, v42, v91\n\t"
  "v_mfma_f32_16x16x32_bf16 v[76:79], v[48:51], v[196:199], v[76:79]\n\t"
  "v_cmp_eq_u32 vcc, -1, v42\n\t"
  "s_nop 0\n\t"
  "v_mfma_f32_16x16x32_bf16 v[76:79], v[52:55], v[200:203], v[76:79]\n\t"
  "s_nop 0\n\t"
  "s_nop 0\n\t"
  "s_waitcnt lgkmcnt(4)\n\t"
  "v_mfma_f32_16x16x32_bf16 v[76:79], v[56:59], v[204:207], v[76:79]\n\t"
  "s_cbranch_vccnz .Lrec_pfix0\n\t"
  ".Lrec_pok0:\n\t"
  "s_waitcnt lgkmcnt(3)\n\t"
  "v_mfma_f32_16x16x32_bf16 v[80:83], v[60:63], v[192:195], 0\n\t"
  "v_mov_b32 v42, 0x3f80\n\t"
  "v_mov_b32 v43, 0x3f800000\n\t"
  "s_waitcnt lgkmcnt(2)\n\t"
  "v_mfma_f32_16x16x32_bf16 v[80:83], v[64:67], v[196:199], v[80:83]\n\t"
  "v_and_b32 v84, 64, v32\n\t"
  "v_cmp_eq_u32 vcc, 0, v84\n\t"
  "s_waitcnt lgkmcnt(1)\n\t"
  "v_mfma_f32_16x16x32_bf16 v[80:83], v[68:71], v[200:203], v[80:83]\n\t"
  "v_cvt_pk_bf16_f32 v0, v76, v77\n\t"
  "v_cvt_pk_bf16_f32 v4, v78, v79\n\t"
  "s_waitcnt lgkmcnt(0)\n\t"
  "v_mfma_f32_16x16x32_bf16 v[80:83], v[72:75], v[204:207], v[80:83]\n\t"
  "v_and_b32 v87, 48, v32\n\t"
  "v_cndmask_b32 v42, v43, v42, vcc\n\t"
  "v_mfma_f32_16x16x32_bf16 v[76:79], v[44:47], v[208:211], 0\n\t"
  "v_lshrrev_b32 v92, 1, v32\n\t"
  "v_cmp_eq_u32 vcc, 0, v87\n\t"
  "v_mfma_f32_16x16x32_bf16 v[76:79], v[48:51], v[212:215], v[76:79]\n\t"
  "v_lshrrev_b32 v93, 2, v32\n\t"
  "v_cndmask_b32 v84, 0, v42, vcc\n\t"
  "v_mfma_f32_16x16x32_bf16 v[76:79], v[52:55], v[216:219], v[76:79]\n\t"
  "v_cvt_pk_bf16_f32 v1, v80, v81\n\t"
  "v_cvt_pk_bf16_f32 v5, v82, v83\n\t"
  "v_mfma_f32_16x16x32_bf16 v[76:79], v[56:59], v[220:223], v[76:79]\n\t"
  "v_and_b32 v93, 64, v93\n\t"
  "v_cmp_eq_u32 vcc, 16, v87\n\t"
  "v_mfma_f32_16x16x32_bf16 v[80:83], v[60:63], v[208:211], 0\n\t"
  "v_and_b32 v92, 56, v92\n\t"
  "v_cndmask_b32 v85, 0, v42, vcc\n\t"
  "v_mfma_f32_16x16x32_bf16 v[80:83], v[64:67], v[212:215], v[80:83]\n\t"
  "s_nop 0\n\t"
  "v_cmp_eq_u32 vcc, 32, v87\n\t"
  "v_mfma_f32_16x16x32_bf16 v[80:83], v[68:71], v[216:219], v[80:83]\n\t"
  "v_cvt_pk_bf16_f32 v8, v76, v77\n\t"
  "v_cvt_pk_bf16_f32 v12, v78, v79\n\t"
  "v_mfma_f32_16x16x32_bf16 v[80:83], v[72:75], v[220:223], v[80:83]\n\t"
  "v_or_b32 v92, v92, v93\n\t"
  "v_cndmask_b32 v86, 0, v42, vcc\n\t"
  "v_mfma_f32_16x16x32_bf16 v[76:79], v[44:47], v[224:227], 0\n\t"
  "s_mov_b32 s66, 0\n\t"
  "v_cmp_eq_u32 vcc, 48, v87\n\t"
  "v_mfma_f32_16x16x32_bf16 v[76:79], v[48:51], v[228:231], v[76:79]\n\t"
  "s_nop 0\n\t"
  "v_cndmask_b32 v87, 0, v42, vcc\n\t"
  "v_mfma_f32_16x16x32_bf16 v[76:79], v[52:55], v[232:235], v[76:79]\n\t"
  "v_cvt_pk_bf16_f32 v9, v80, v81\n\t"
  "v_cvt_pk_bf16_f32 v13, v82, v83\n\t"
  "v_mfma_f32_16x16x32_bf16 v[76:79], v[56:59], v[236:239], v[76:79]\n\t"
  "v_mfma_f32_16x16x32_bf16 v[80:83], v[60:63], v[224:227], 0\n\t"
  "v_mfma_f32_16x16x32_bf16 v[80:83], v[64:67], v[228:231], v[80:83]\n\t"
  "v_mfma_f32_16x16x32_bf16 v[80:83], v[68:71], v[232:235], v[80:83]\n\t"
  "v_cvt_pk_bf16_f32 v16, v76, v77\n\t"
  "v_cvt_pk_bf16_f32 v20, v78, v79\n\t"
  "v_mfma_f32_16x16x32_bf16 v[80:83], v[72:75], v[236:239], v[80:83]\n\t"
  ".Lrec_red:\n\t"
  "s_waitcnt vmcnt(3)\n\t"
  "v_mfma_f32_16x16x32_bf16 v[116:119], v[84:87], v[100:103], 0\n\t"
  "v_max3_u32 v42, v100, v101, v102\n\t"
  "v_max_u32 v42, v42, v103\n\t"
  "s_waitcnt vmcnt(2)\n\t"
  "v_mfma_f32_16x16x32_bf16 v[116:119], v[84:87], v[104:107], v[116:119]\n\t"
  "v_max3_u32 v42, v104, v105, v42\n\t"
  "v_max3_u32 v42, v106, v107, v42\n\t"
  "s_waitcnt vmcnt(1)\n\t"
  "v_mfma_f32_16x16x32_bf16 v[116:119], v[84:87], v[108:111], v[116:119]\n\t"
  "v_max3_u32 v42, v108, v109, v42\n\t"
  "v_max3_u32 v42, v110, v111, v42\n\t"
  "s_waitcnt vmcnt(0)\n\t"
  "v_mfma_f32_16x16x32_bf16 v[116:119], v[84:87], v[112:115], v[116:119]\n\t"
  "v_max3_u32 v42, v112, v113, v42\n\t"
  "v_max3_u32 v42, v114, v115, v42\n\t"
  "v_cmp_eq_u32 vcc, -1, v42\n\t"
  "s_cbranch_vccnz .Lrec_pre\n\t"
  "s_nop 3\n\t"
  "v_add_f32_dpp v116, v116, v116 row_ror:8 row_mask:0xf bank_mask:0xf\n\t"
  "v_add_f32_dpp v117, v117, v117 row_ror:8 row_mask:0xf bank_mask:0xf\n\t"
  "v_add_f32_dpp v118, v118, v118 row_ror:8 row_mask:0xf bank_mask:0xf\n\t"
  "v_add_f32_dpp v119, v119, v119 row_ror:8 row_mask:0xf bank_mask:0xf\n\t"
  "v_cvt_pk_bf16_f32 v116, v116, v117\n\t"
  "v_cvt_pk_bf16_f32 v117, v118, v119\n\t"
  "s_mov_b64 exec, 0x00ff00ff\n\t"
  "v_min_u32 v116, -2, v116\n\t"
  "v_min_u32 v117, -2, v117\n\t"
  "buffer_store_dwordx2 v[116:117], v92, s[40:43], s29 offen sc1\n\t"
  "s_mov_b64 exec, -1\n\t"
  "v_mfma_f32_16x16x32_bf16 v[76:79], v[44:47], v[240:243], 0\n\t"
  "s_add_u32 s60, s60, 1\n\t"
  "s_add_u32 s44, s44, 0x4000\n\t"
  "v_mfma_f32_16x16x32_bf16 v[76:79], v[48:51], v[244:247], v[76:79]\n\t"
  "s_add_u32 s61, s61, 0x4000\n\t"
  "s_add_u32 s29, s29, 0x4000\n\t"
  "v_mfma_f32_16x16x32_bf16 v[76:79], v[52:55], v[248:251], v[76:79]\n\t"
  "v_cvt_pk_bf16_f32 v17, v80, v81\n\t"
  "v_cvt_pk_bf16_f32 v21, v82, v83\n\t"
  "v_mfma_f32_16x16x32_bf16 v[76:79], v[56:59], v[252:255], v[76:79]\n\t"
  "s_lshr_b32 s78, s48, 21\n\t"
  "s_bitcmp1_b64 s[76:77], s78\n\t"
  "v_mfma_f32_16x16x32_bf16 v[80:83], v[60:63], v[240:243], 0\n\t"
  "s_cselect_b32 s78, 0x2000, 0\n\t"
  "s_add_u32 s75, s48, s78\n\t"
  "v_mfma_f32_16x16x32_bf16 v[80:83], v[64:67], v[244:247], v[80:83]\n\t"
  "s_add_u32 s48, s48, 0x40000\n\t"
  "s_add_u32 s28, s28, 0x4000\n\t"
  "v_mfma_f32_16x16x32_bf16 v[80:83], v[68:71], v[248:251], v[80:83]\n\t"
  "v_cvt_pk_bf16_f32 v24, v76, v77\n\t"
  "v_cvt_pk_bf16_f32 v28, v78, v79\n\t"
  "v_mfma_f32_16x16x32_bf16 v[80:83], v[72:75], v[252:255], v[80:83]\n\t"
  "s_memtime s[24:25]\n\t"
  "s_nop 6\n\t"
  "v_cvt_pk_bf16_f32 v25, v80, v81\n\t"
  "v_cvt_pk_bf16_f32 v29, v82, v83\n\t"
  "s_waitcnt lgkmcnt(0)\n\t"
  "s_sub_u32 s67, s24, s22\n\t"
  "s_sub_u32 s67, s67, s27\n\t"
  "s_sub_u32 s67, s67, 2550\n\t"
  "s_ashr_i32 s67, s67, 2\n\t"
  "s_add_u32 s67, s67, s20\n\t"
  "s_mov_b32 s66, 0\n\t"
  ".Lrec_ulb:\n\t"
  "v_cmp_lt_u32 vcc, s67, v96\n\t"
  "s_add_u32 s67, s67, 9\n\t"
  "s_cbranch_vccz .Lrec_ugb\n\t"
  "v_cmp_lt_u32 vcc, s67, v96\n\t"
  "s_add_u32 s67, s67, 9\n\t"
  "s_cbranch_vccz .Lrec_ugb\n\t"
  "v_cmp_lt_u32 vcc, s67, v96\n\t"
  "s_add_u32 s67, s67, 9\n\t"
  "s_cbranch_vccz .Lrec_ugb\n\t"
  "v_cmp_lt_u32 vcc, s67, v96\n\t"
  "s_add_u32 s67, s67, 9\n\t"
  "s_cbranch_vccz .Lrec_ugb\n\t"
  "v_cmp_lt_u32 vcc, s67, v96\n\t"
  "s_add_u32 s67, s67, 9\n\t"
  "s_cbranch_vccz .Lrec_ugb\n\t"
  "v_cmp_lt_u32 vcc, s67, v96\n\t"
  "s_add_u32 s67, s67, 9\n\t"
  "s_cbranch_vccz .Lrec_ugb\n\t"
  "v_cmp_lt_u32 vcc, s67, v96\n\t"
  "s_add_u32 s67, s67, 9\n\t"
  "s_cbranch_vccz .Lrec_ugb\n\t"
  "v_cmp_lt_u32 vcc, s67, v96\n\t"
  "s_add_u32 s67, s67, 9\n\t"
  "s_cbranch_vccz .Lrec_ugb\n\t"
  "v_cmp_lt_u32 vcc, s67, v96\n\t"
  "s_add_u32 s67, s67, 9\n\t"
  "s_cbranch_vccz .Lrec_ugb\n\t"
  "v_cmp_lt_u32 vcc, s67, v96\n\t"
  "s_add_u32 s67, s67, 9\n\t"
  "s_cbranch_vccz .Lrec_ugb\n\t"
  "s_add_u32 s67, s67, 9\n\t"
  "s_add_u32 s66, s66, 1\n\t"
  "s_cmp_lt_u32 s66, 30\n\t"
  "s_cbranch_scc1 .Lrec_ulb\n\t"
  ".Lrec_ugb:\n\t"
  ".Lrec_b4:\n\t"
  "s_barrier\n\t"
  "s_branch .Lrec_top\n\t"
  ".Lrec_oth:\n\t"
  "ds_read_b128 v[56:59], v43 offset:3072\n\t"
  "ds_read_b128 v[60:63], v43 offset:4096\n\t"
  "ds_read_b128 v[64:67], v43 offset:5120\n\t"
  "ds_read_b128 v[68:71], v43 offset:6144\n\t"
  "ds_read_b128 v[72:75], v43 offset:7168\n\t"
  "v_max3_u32 v42, v88, v89, v90\n\t"
  "v_max_u32 v42, v42, v91\n\t"
  "v_cmp_eq_u32 vcc, -1, v42\n\t"
  "s_cbranch_vccnz .Lrec_pfix1\n\t"
  ".Lrec_pok1:\n\t"
  "s_add_u32 s21, s60, 2\n\t"
  "s_barrier\n\t"
  "s_cmp_lg_u32 s60, 0\n\t"
  "s_cbranch_scc1 .Lrec_kvokc\n\t"
  "s_waitcnt lgkmcnt(0)\n\t"
  "s_mov_b32 s66, 0\n\t"
  ".Lrec_kvc:\n\t"
  "v_and_b32 v22, 16, v32\n\t"
  "v_lshlrev_b32 v22, 8, v22\n\t"
  "buffer_load_dword v26, v22, s[40:43], s51 offen sc1\n\t"
  "s_waitcnt vmcnt(0)\n\t"
  "v_cmp_gt_u32 vcc, 3, v26\n\t"
  "s_cbranch_vccz .Lrec_kvokc\n\t"
  "s_sleep 1\n\t"
  "s_add_u32 s66, s66, 1\n\t"
  "s_and_b32 s67, s66, 255\n\t"
  "s_cmp_lg_u32 s67, 0\n\t"
  "s_cbranch_scc1 .Lrec_kvc\n\t"
  "buffer_load_dword v18, off, s[40:43], s54 sc1\n\t"
  "s_waitcnt vmcnt(0)\n\t"
  "v_readfirstlane_b32 s67, v18\n\t"
  "s_nop 1\n\t"
  "s_cmp_lg_u32 s67, 0\n\t"
  "s_cbranch_scc1 .Lrec_end\n\t"
  "s_cmp_lt_u32 s66, 0x80000\n\t"
  "s_cbranch_scc1 .Lrec_kvc\n\t"
  "v_mov_b32 v18, 1\n\t"
  "buffer_store_dword v18, off, s[40:43], s54 sc1\n\t"
  "s_branch .Lrec_end\n\t"
  ".Lrec_kvokc:\n\t"
  "s_and_b32 s68, s21, 3\n\t"
  "s_lshl_b32 s68, s68, 14\n\t"
  "s_add_u32 m0, s46, s68\n\t"
  "s_min_u32 s67, s21, 63\n\t"
  "s_lshl_b32 s67, s67, 19\n\t"
  "s_add_u32 s62, s45, s67\n\t"
  "s_lshr_b32 s78, s62, 21\n\t"
  "s_bitcmp1_b64 s[76:77], s78\n\t"
  "s_cselect_b32 s78, 0x2000, 0\n\t"
  "s_add_u32 s62, s62, s78\n\t"
  "buffer_load_dwordx4 v32, s[40:43], s62 offen offset:0 sc1 lds\n\t"
  "buffer_load_dwordx4 v32, s[40:43], s62 offen offset:1024 sc1 lds\n\t"
  "buffer_load_dwordx4 v32, s[40:43], s62 offen offset:2048 sc1 lds\n\t"
  "buffer_load_dwordx4 v32, s[40:43], s62 offen offset:3072 sc1 lds\n\t"
  "s_cmp_eq_u32 s49, 3\n\t"
  "s_cbranch_scc1 .Lrec_ndl\n\t"
  "s_add_u32 m0, s38, s68\n\t"
  "s_add_u32 s62, s31, s67\n\t"
  "s_lshr_b32 s78, s62, 21\n\t"
  "s_bitcmp1_b64 s[76:77], s78\n\t"
  "s_cselect_b32 s78, 0x2000, 0\n\t"
  "s_add_u32 s62, s62, s78\n\t"
  "buffer_load_dwordx4 v32, s[40:43], s62 offen offset:0 sc1 lds\n\t"
  "buffer_load_dwordx4 v32, s[40:43], s62 offen offset:1024 sc1 lds\n\t"
  ".Lrec_ndl:\n\t"
  "s_add_u32 s21, s60, 4\n\t"
  "s_min_u32 s21, s21, 64\n\t"
  "s_cmp_eq_u32 s49, 3\n\t"
  "s_cselect_b32 s21, s21, 0\n\t"
  "s_cmp_ge_u32 s39, s21\n\t"
  "s_cbranch_scc1 .Lrec_join\n\t"
  "v_and_b32 v22, 16, v32\n\t"
  "v_lshlrev_b32 v22, 8, v22\n\t"
  "buffer_load_dword v26, v22, s[40:43], s51 offen sc1\n\t"
  ".Lrec_join:\n\t"
  "s_waitcnt lgkmcnt(0)\n\t"
  "v_mfma_f32_16x16x32_bf16 v[76:79], v[44:47], v[192:195], 0\n\t"
  "v_mfma_f32_16x16x32_bf16 v[76:79], v[48:51], v[196:199], v[76:79]\n\t"
  "v_mfma_f32_16x16x32_bf16 v[76:79], v[52:55], v[200:203], v[76:79]\n\t"
  "v_mfma_f32_16x16x32_bf16 v[76:79], v[56:59], v[204:207], v[76:79]\n\t"
  "v_mfma_f32_16x16x32_bf16 v[80:83], v[60:63], v[192:195], 0\n\t"
  "v_mfma_f32_16x16x32_bf16 v[80:83], v[64:67], v[196:199], v[80:83]\n\t"
  "v_mfma_f32_16x16x32_bf16 v[80:83], v[68:71], v[200:203], v[80:83]\n\t"
  "v_cvt_pk_bf16_f32 v0, v76, v77\n\t"
  "v_cvt_pk_bf16_f32 v4, v78, v79\n\t"
  "v_mfma_f32_16x16x32_bf16 v[80:83], v[72:75], v[204:207], v[80:83]\n\t"
  "v_mfma_f32_16x16x32_bf16 v[76:79], v[44:47], v[208:211], 0\n\t"
  "v_mfma_f32_16x16x32_bf16 v[76:79], v[48:51], v[212:215], v[76:79]\n\t"
  "v_mfma_f32_16x16x32_bf16 v[76:79], v[52:55], v[216:219], v[76:79]\n\t"
  "v_cvt_pk_bf16_f32 v1, v80, v81\n\t"
  "v_cvt_pk_bf16_f32 v5, v82, v83\n\t"
  "v_mfma_f32_16x16x32_bf16 v[76:79], v[56:59], v[220:223], v[76:79]\n\t"
  "v_mfma_f32_16x16x32_bf16 v[80:83], v[60:63], v[208:211], 0\n\t"
  "v_mfma_f32_16x16x32_bf16 v[80:83], v[64:67], v[212:215], v[80:83]\n\t"
  "v_mfma_f32_16x16x32_bf16 v[80:83], v[68:71], v[216:219], v[80:83]\n\t"
  "v_cvt_pk_bf16_f32 v8, v76, v77\n\t"
  "v_cvt_pk_bf16_f32 v12, v78, v79\n\t"
  "v_mfma_f32_16x16x32_bf16 v[80:83], v[72:75], v[220:223], v[80:83]\n\t"
  "v_mfma_f32_16x16x32_bf16 v[76:79], v[44:47], v[224:227], 0\n\t"
  "v_mfma_f32_16x16x32_bf16 v[76:79], v[48:51], v[228:231], v[76:79]\n\t"
  "v_mfma_f32_16x16x32_bf16 v[76:79], v[52:55], v[232:235], v[76:79]\n\t"
  "v_cvt_pk_bf16_f32 v9, v80, v81\n\t"
  "v_cvt_pk_bf16_f32 v13, v82, v83\n\t"
  "v_mfma_f32_16x16x32_bf16 v[76:79], v[56:59], v[236:239], v[76:79]\n\t"
  "v_mfma_f32_16x16x32_bf16 v[80:83], v[60:63], v[224:227], 0\n\t"
  "v_mfma_f32_16x16x32_bf16 v[80:83], v[64:67], v[228:231], v[80:83]\n\t"
  "v_mfma_f32_16x16x32_bf16 v[80:83], v[68:71], v[232:235], v[80:83]\n\t"
  "v_cvt_pk_bf16_f32 v16, v76, v77\n\t"
  "v_cvt_pk_bf16_f32 v20, v78, v79\n\t"
  "v_mfma_f32_16x16x32_bf16 v[80:83], v[72:75], v[236:239], v[80:83]\n\t"
  "v_mfma_f32_16x16x32_bf16 v[76:79], v[44:47], v[240:243], 0\n\t"
  "v_mfma_f32_16x16x32_bf16 v[76:79], v[48:51], v[244:247], v[76:79]\n\t"
  "v_mfma_f32_16x16x32_bf16 v[76:79], v[52:55], v[248:251], v[76:79]\n\t"
  "v_cvt_pk_bf16_f32 v17, v80, v81\n\t"
  "v_cvt_pk_bf16_f32 v21, v82, v83\n\t"
  "v_mfma_f32_16x16x32_bf16 v[76:79], v[56:59], v[252:255], v[76:79]\n\t"
  "v_mfma_f32_16x16x32_bf16 v[80:83], v[60:63], v[240:243], 0\n\t"
  "v_mfma_f32_16x16x32_bf16 v[80:83], v[64:67], v[244:247], v[80:83]\n\t"
  "v_mfma_f32_16x16x32_bf16 v[80:83], v[68:71], v[248:251], v[80:83]\n\t"
  "v_cvt_pk_bf16_f32 v24, v76, v77\n\t"
  "v_cvt_pk_bf16_f32 v28, v78, v79\n\t"
  "v_mfma_f32_16x16x32_bf16 v[80:83], v[72:75], v[252:255], v[80:83]\n\t"
  "s_nop 7\n\t"
  "v_cvt_pk_bf16_f32 v25, v80, v81\n\t"
  "v_cvt_pk_bf16_f32 v29, v82, v83\n\t"
  "s_add_u32 s60, s60, 1\n\t"
  "s_add_u32 s44, s44, 0x4000\n\t"
  "s_add_u32 s61, s61, 0x4000\n\t"
  "s_add_u32 s29, s29, 0x4000\n\t"
  "s_lshr_b32 s78, s48, 21\n\t"
  "s_bitcmp1_b64 s[76:77], s78\n\t"
  "s_cselect_b32 s78, 0x2000, 0\n\t"
  "s_add_u32 s75, s48, s78\n\t"
  "s_add_u32 s48, s48, 0x40000\n\t"
  "s_add_u32 s28, s28, 0x4000\n\t"
  "s_cmp_ge_u32 s39, s21\n\t"
  "s_cbranch_scc1 .Lrec_b4\n\t"
  "s_waitcnt vmcnt(0)\n\t"
  "v_cmp_gt_u32 vcc, s21, v26\n\t"
  "s_cbranch_vccz .Lrec_kvokd\n\t"
  "s_mov_b32 s66, 0\n\t"
  ".Lrec_kvd:\n\t"
  "v_and_b32 v22, 16, v32\n\t"
  "v_lshlrev_b32 v22, 8, v22\n\t"
  "buffer_load_dword v26, v22, s[40:43], s51 offen sc1\n\t"
  "s_waitcnt vmcnt(0)\n\t"
  "v_cmp_gt_u32 vcc, s21, v26\n\t"
  "s_cbranch_vccz .Lrec_kvokd\n\t"
  "s_sleep 1\n\t"
  "s_add_u32 s66, s66, 1\n\t"
  "s_and_b32 s67, s66, 255\n\t"
  "s_cmp_lg_u32 s67, 0\n\t"
  "s_cbranch_scc1 .Lrec_kvd\n\t"
  "buffer_load_dword v18, off, s[40:43], s54 sc1\n\t"
  "s_waitcnt vmcnt(0)\n\t"
  "v_readfirstlane_b32 s67, v18\n\t"
  "s_nop 1\n\t"
  "s_cmp_lg_u32 s67, 0\n\t"
  "s_cbranch_scc1 .Lrec_end\n\t"
  "s_cmp_lt_u32 s66, 0x80000\n\t"
  "s_cbranch_scc1 .Lrec_kvd\n\t"
  "v_mov_b32 v18, 1\n\t"
  "buffer_store_dword v18, off, s[40:43], s54 sc1\n\t"
  "s_branch .Lrec_end\n\t"
  ".Lrec_kvokd:\n\t"
  "v_readlane_b32 s39, v26, 0\n\t"
  "v_readlane_b32 s67, v26, 1\n\t"
  "s_min_u32 s39, s39, s67\n\t"
  "s_branch .Lrec_b4\n\t"
  ".Lrec_mre:\n\t"
  "s_mov_b32 s66, 0\n\t"
  "s_waitcnt lgkmcnt(0)\n\t"
  ".Lrec_mpoll:\n\t"
  "buffer_load_dwordx4 v[96:99], v34, s[40:43], s44 offen sc1\n\t"
  "buffer_load_dwordx4 v[100:103], v34, s[40:43], s61 offen sc1\n\t"
  "s_waitcnt vmcnt(0)\n\t"
  "v_max3_u32 v42, v96, v97, v98\n\t"
  "v_max3_u32 v42, v42, v99, v100\n\t"
  "v_max3_u32 v42, v42, v101, v102\n\t"
  "v_max_u32 v42, v42, v103\n\t"
  "v_cmp_eq_u32 vcc, -1, v42\n\t"
  "s_cbranch_vccz .Lrec_init\n\t"
  "s_add_u32 s66, s66, 1\n\t"
  "s_and_b32 s67, s66, 255\n\t"
  "s_cmp_lg_u32 s67, 0\n\t"
  "s_cbranch_scc1 .Lrec_mpoll\n\t"
  "buffer_load_dword v18, off, s[40:43], s54 sc1\n\t"
  "s_waitcnt vmcnt(0)\n\t"
  "v_readfirstlane_b32 s67, v18\n\t"
  "s_nop 1\n\t"
  "s_cmp_lg_u32 s67, 0\n\t"
  "s_cbranch_scc1 .Lrec_end\n\t"
  "s_cmp_lt_u32 s66, 0x80000\n\t"
  "s_cbranch_scc1 .Lrec_mpoll\n\t"
  "v_mov_b32 v18, 1\n\t"
  "buffer_store_dword v18, off, s[40:43], s54 sc1\n\t"
  "s_branch .Lrec_end\n\t"
  ".Lrec_init:\n\t"
  "ds_read_b128 v[44:47], v40 offset:0\n\t"
  "ds_read_b128 v[48:51], v40 offset:1024\n\t"
  "v_mov_b64 v[72:73], 0\n\t"
  "v_mov_b64 v[74:75], 0\n\t"
  "v_mov_b64 v[76:77], 0\n\t"
  "v_mov_b64 v[78:79], 0\n\t"
  "ds_read_b128 v[52:55], v40 offset:2048\n\t"
  "ds_read_b128 v[56:59], v40 offset:3072\n\t"
  "v_mov_b64 v[80:81], 0\n\t"
  "v_mov_b64 v[82:83], 0\n\t"
  "v_mov_b64 v[84:85], 0\n\t"
  "v_mov_b64 v[86:87], 0\n\t"
  "ds_read_b128 v[60:63], v40 offset:4096\n\t"
  "ds_read_b128 v[64:67], v40 offset:5120\n\t"
  "v_mov_b64 v[88:89], 0\n\t"
  "v_mov_b64 v[90:91], 0\n\t"
  "v_mov_b64 v[92:93], 0\n\t"
  "v_mov_b64 v[94:95], 0\n\t"
  "s_waitcnt lgkmcnt(0)\n\t"
  "s_branch .Lrec_mgo\n\t"
  ".Lrec_pre:\n\t"
  "buffer_load_dwordx4 v[100:103], v32, s[40:43], s64 offen offset:0 sc1\n\t"
  "buffer_load_dwordx4 v[104:107], v32, s[40:43], s64 offen offset:1024 sc1\n\t"
  "buffer_load_dwordx4 v[108:111], v32, s[40:43], s64 offen offset:2048 sc1\n\t"
  "buffer_load_dwordx4 v[112:115], v32, s[40:43], s64 offen offset:3072 sc1\n\t"
  "s_add_u32 s66, s66, 1\n\t"
  "s_and_b32 s67, s66, 255\n\t"
  "s_cmp_lg_u32 s67, 0\n\t"
  "s_cbranch_scc1 .Lrec_red\n\t"
  "buffer_load_dword v18, off, s[40:43], s54 sc1\n\t"
  "s_waitcnt vmcnt(0)\n\t"
  "v_readfirstlane_b32 s67, v18\n\t"
  "s_nop 1\n\t"
  "s_cmp_lg_u32 s67, 0\n\t"
  "s_cbranch_scc1 .Lrec_end\n\t"
  "s_cmp_lt_u32 s66, 0x80000\n\t"
  "s_cbranch_scc1 .Lrec_red\n\t"
  "v_mov_b32 v18, 1\n\t"
  "buffer_store_dword v18, off, s[40:43], s54 sc1\n\t"
  "s_branch .Lrec_end\n\t"
  ".Lrec_tbad:\n\t"
  "v_cmp_eq_u32 vcc, 0, v96\n\t"
  "s_cbranch_vccz .Lrec_tfar\n\t"
  "v_lshlrev_b32 v97, 3, v32\n\t"
  "buffer_load_dword v96, v97, s[40:43], s28 offen sc1\n\t"
  "s_waitcnt vmcnt(0) lgkmcnt(0)\n\t"
  "s_add_u32 s66, s66, 1\n\t"
  "s_and_b32 s67, s66, 255\n\t"
  "s_cmp_lg_u32 s67, 0\n\t"
  "s_cbranch_scc1 .Lrec_ts\n\t"
  "buffer_load_dword v18, off, s[40:43], s54 sc1\n\t"
  "s_waitcnt vmcnt(0)\n\t"
  "v_readfirstlane_b32 s67, v18\n\t"
  "s_nop 1\n\t"
  "s_cmp_lg_u32 s67, 0\n\t"
  "s_cbranch_scc1 .Lrec_end\n\t"
  "s_cmp_lt_u32 s66, 0x80000\n\t"
  "s_cbranch_scc1 .Lrec_ts\n\t"
  "v_mov_b32 v18, 1\n\t"
  "buffer_store_dword v18, off, s[40:43], s54 sc1\n\t"
  "s_branch .Lrec_end\n\t"
  ".Lrec_tfar:\n\t"
  "s_mov_b32 s27, 1000\n\t"
  "s_branch .Lrec_tok\n\t"
  ".Lrec_pfix0:\n\t"
  "s_nop 1\n\t"
  "v_min_u32 v88, -2, v88\n\t"
  "v_min_u32 v89, -2, v89\n\t"
  "v_min_u32 v90, -2, v90\n\t"
  "v_min_u32 v91, -2, v91\n\t"
  "buffer_store_dwordx4 v[88:91], v41, s[40:43], s65 offen sc1\n\t"
  "s_waitcnt vmcnt(0)\n\t"
  "s_branch .Lrec_pok0\n\t"
  ".Lrec_pfix1:\n\t"
  "s_nop 1\n\t"
  "v_min_u32 v88, -2, v88\n\t"
  "v_min_u32 v89, -2, v89\n\t"
  "v_min_u32 v90, -2, v90\n\t"
  "v_min_u32 v91, -2, v91\n\t"
  "buffer_store_dwordx4 v[88:91], v41, s[40:43], s65 offen sc1\n\t"
  "s_waitcnt vmcnt(0)\n\t"
  "s_branch .Lrec_pok1\n\t"
  ".Lrec_last:\n\t"
  "s_waitcnt lgkmcnt(0)\n\t"
  "s_nop 7\n\t"
  "v_lshrrev_b32 v70, 2, v32\n\t"
  "v_bfe_u32 v71, v32, 4, 1\n\t"
  "v_and_b32 v70, 0xf8, v70\n\t"
  "v_mad_u32_u24 v70, v71, s57, v70\n\t"
  "v_add_u32 v70, s53, v70\n\t"
  "v_add_u32 v71, s30, v70\n\t"
  "ds_read_b32 v68, v70\n\t"
  "ds_read_b32 v69, v71\n\t"
  "v_lshrrev_b32 v70, 1, v32\n\t"
  "s_lshr_b32 s78, s48, 21\n\t"
  "s_bitcmp1_b64 s[76:77], s78\n\t"
  "s_cselect_b32 s78, 0x2000, 0\n\t"
  "s_add_u32 s48, s48, s78\n\t"
  "s_waitcnt lgkmcnt(0)\n\t"
  "buffer_store_dwordx2 v[68:69], v70, s[40:43], s48 offen sc1\n\t"
  "v_mov_b32 v26, 64\n\t"
  "s_waitcnt vmcnt(0)\n\t"
  "s_barrier\n\t"
  "s_mov_b64 exec, 1\n\t"
  "buffer_store_dword v26, off, s[40:43], s50 sc1\n\t"
  "s_mov_b64 exec, -1\n\t"
  ".Lrec_end:\n\t"
  "s_waitcnt vmcnt(0) lgkmcnt(0)\n\t"
    : "+{v[32:47]}"(cv), "+{s[44:59]}"(cs)
    : "{a[0:31]}"(W[0]), "{a[32:63]}"(W[1]), "{a[64:95]}"(W[2]), "{a[96:127]}"(W[3]), "{a[128:159]}"(W[4]), "{a[160:191]}"(W[5]), "{a[192:223]}"(W[6]), "{a[224:255]}"(W[7]),
      "{v[128:159]}"(W[8]), "{v[160:191]}"(W[9]), "{v[192:223]}"(W[10]), "{v[224:255]}"(W[11]), "{s[40:43]}"(rw), "{s[76:77]}"(nr.p0)
    : "memory", "vcc", "scc", "v0", "v1", "v2", "v3", "v4", "v5", "v6", "v7", "v8", "v9", "v10", "v11", "v12", "v13", "v14", "v15", "v16", "v17", "v18", "v19", "v20", "v21", "v22", "v23", "v24", "v25", "v26", "v27", "v28", "v29", "v30", "v31", "v48", "v49", "v50", "v51", "v52", "v53", "v54", "v55", "v56", "v57", "v58", "v59", "v60", "v61", "v62", "v63", "v64", "v65", "v66", "v67", "v68", "v69", "v70", "v71", "v72", "v73", "v74", "v75", "v76", "v77", "v78", "v79", "v80", "v81", "v82", "v83", "v84", "v85", "v86", "v87", "v88", "v89", "v90", "v91", "v92", "v93", "v94", "v95", "v96", "v97", "v98", "v99", "v100", "v101", "v102", "v103", "v104", "v105", "v106", "v107", "v108", "v109", "v110", "v111", "v112", "v113", "v114", "v115", "v116", "v117", "v118", "v119", "v120", "v121", "v122", "v123", "v124", "v125", "v126", "v127", "s8", "s9", "s10", "s11", "s12", "s13", "s14", "s15", "s16", "s17", "s18", "s19", "s20", "s21", "s22", "s23", "s24", "s25", "s26", "s27", "s28", "s29", "s30", "s31", "s34", "s35", "s36", "s37", "s38", "s39", "s60", "s61", "s62", "s63", "s64", "s65", "s66", "s67", "s68", "s69", "s70", "s71", "s72", "s73", "s74", "s75", "s78", "s79", "s80", "s81", "s82", "s83", "s84", "s85", "s86", "s87", "s88", "s89", "s90", "s91", "s92", "s93", "s94", "s95", "s96", "s97", "s98", "s99");
  __builtin_amdgcn_endpgm();
}

template <int NK, int D0> DEV void read_cols128(u32x32 (&A)[8], int k0, lds_t base, int w, int lane) {
  const int gb = lane >> 4, qq = (lane >> 2) & 3, pp = lane & 3;
  const unsigned rid = (unsigned)(qq | ((gb & 1) << 2));
#pragma unroll
  for (int t = 0; t < 8; ++t) {
    const unsigned chunk = (unsigned)(w * 16 + (t >> 1) * 4 + pp) ^ (rid << 1);
    const lds_t b = base + (8 * gb + qq) * 1024 + chunk * 16 + (t & 1) * 8;
#pragma unroll
    for (int u = 0; u < 4; ++u) {
      const u32x2 f = ldstr(b + (u >> 1) * 32768 + (u & 1) * 4096);
      const int d = D0 + (t * NK + k0) * 4 + u * 2;
      A[d >> 5][d & 31] = f.x; A[d >> 5][(d & 31) + 1] = f.y;
    }
  }
}
DEV void role_att(const Params& p, char* wb, rsrc_t rw, int b, lds_t L, int tid) {
  const int lane = tid & 63, w = __builtin_amdgcn_readfirstlane(tid >> 6);
  const unsigned l0 = (unsigned)(uintptr_t)L;
  const int gb = lane >> 4, qq = (lane >> 2) & 3, pp = lane & 3, n16 = lane & 15;
  const int hp = b >> 1, tt = b & 1, hh = w >> 1, h = 2 * hp + hh, eh = w & 1;
  u32x32 A[8];
  const char* wo = (const char*)(p.wo + (size_t)hp * 256 * 512);
  stage_chunk<1024, 0>(wo, 1024, l0, w, lane);
  stage_chunk<1024, 0>(wo + 65536, 1024, l0 + 65536, w, lane);
#pragma unroll
  for (int ck = 0; ck < 4; ++ck) {
    if (ck < 3) VMWAIT(16); else VMWAIT(0);
    BARRIER();
    read_cols128<8, 0>(A, 2 * ck, L + (ck & 1) * 65536, w, lane);
    BARRIER();
    if (ck < 2) stage_chunk<1024, 0>(wo + (ck + 2) * 65536, 1024, l0 + (ck & 1) * 65536, w, lane);
  }
  const Near nr = get_near(rw, lane);
  u32x16 cv = {}, cs = {}, cm = {};
  cv[0] = (unsigned)lane * 16;
  cv[1] = l0 + (unsigned)(hh * 20480 + lane * 16);
  cv[2] = hm((unsigned)(((tt * 4 + (n16 >> 2)) * 16 + hp) * 4096 + (16 * w + gb) * 64 + (n16 & 3) * 16));
  cv[3] = fa(nr, F_REC + 2 * h + (lane & 1));
  cv[4] = fa0(F_KV + min(lane, 2) * 16 + hp);
  cv[5] = l0 + (unsigned)(hh * 20480 + 12288 + eh * 2048 + (pp * 16 + 4 * gb + qq) * 16);
  cv[6] = l0 + (unsigned)(98304 + w * 2048 + (8 * gb + qq) * 16 + (pp & 1) * 8);
  cv[7] = (unsigned)((64 * eh + 8 * (n16 >> 2) + (n16 & 3)) * 16 + (gb & 1) * 8);
  cv[8] = l0 + (unsigned)(81920 + lane * 16);
  cs[0] = OFF_QKV + (unsigned)(h * 16384 + eh * 2048);
  cs[1] = l0 + (unsigned)(hh * 20480 + eh * 2048);
  cs[2] = OFF_SEG;
  cs[12] = (unsigned)nr.p0; cs[13] = (unsigned)(nr.p0 >> 32); cs[14] = (unsigned)nr.ps; cs[15] = (unsigned)(nr.ps >> 32);
  cs[3] = fa(nr, F_ATT + b) + 64 * (unsigned)w;
  cs[4] = OFF_KVM + hm((unsigned)h * 4096);
  cs[5] = OFF_CTRL + C_ERR;
  cs[6] = l0 + (unsigned)(98304 + w * 2048);
  cs[7] = w == 0 ? OFF_DBG + (unsigned)((144 + b) * 256 + 8) * 8 : 0xf0000000u;
  cs[8] = (unsigned)tt * 4096;
  cs[9] = (unsigned)w * 2048;
  cs[11] = f2u(0.08838834764831845f * 1.4426950408889634f);
#pragma unroll
  for (int r = 0; r < 4; ++r) {
    const unsigned long long fut = __ballot(4 * gb + r > n16);
    const unsigned long long m0 = tt == 0 ? fut : 0ull, m1 = tt == 0 ? ~0ull : fut;
    cm[2 * r] = (unsigned)m0; cm[2 * r + 1] = (unsigned)(m0 >> 32); cm[8 + 2 * r] = (unsigned)m1; cm[9 + 2 * r] = (unsigned)(m1 >> 32);
  }
  asm volatile(
  "s_mov_b32 s83, 0\n\t"
  "s_mov_b32 s84, 0\n\t"
  "s_mov_b32 s85, 0\n\t"
  "v_mov_b32 v12, 0\n\t"
  "v_mov_b32 v123, 0xd028965f\n\t"
  "s_cmp_eq_u32 s52, 0\n\t"
  "s_cselect_b32 s95, 0xf0000000, 0\n\t"
  "s_mov_b32 s92, 0\n\t"
  "s_min_u32 s91, s92, 63\n\t"
  "v_cmp_ge_i32 vcc, s91, v12\n\t"
  "s_cbranch_vccz .Latt_qaok\n\t"
  "s_mov_b32 s80, 0\n\t"
  ".Latt_qa:\n\t"
  "buffer_load_dword v12, v4, s[40:43], s54 offen sc1\n\t"
  "s_waitcnt vmcnt(0)\n\t"
  "v_cmp_ge_i32 vcc, s91, v12\n\t"
  "s_cbranch_vccz .Latt_qaok\n\t"
  "s_sleep 3\n\t"
  "s_add_u32 s80, s80, 1\n\t"
  "s_and_b32 s81, s80, 255\n\t"
  "s_cmp_lg_u32 s81, 0\n\t"
  "s_cbranch_scc1 .Latt_qa\n\t"
  "buffer_load_dword v9, off, s[40:43], s49 sc1\n\t"
  "s_waitcnt vmcnt(0)\n\t"
  "v_readfirstlane_b32 s81, v9\n\t"
  "s_nop 1\n\t"
  "s_cmp_lg_u32 s81, 0\n\t"
  "s_cbranch_scc1 .Latt_end\n\t"
  "s_cmp_lt_u32 s80, 0x80000\n\t"
  "s_cbranch_scc1 .Latt_qa\n\t"
  "v_mov_b32 v9, 1\n\t"
  "buffer_store_dword v9, off, s[40:43], s49 sc1\n\t"
  "s_branch .Latt_end\n\t"
  ".Latt_qaok:\n\t"
  "s_lshl_b32 s88, s92, 19\n\t"
  "s_add_u32 s88, s88, s44\n\t"
  "s_cmp_lt_u32 s92, 64\n\t"
  "s_cselect_b32 s88, s88, 0xe0000000\n\t"
  "s_and_b32 s89, s92, 1\n\t"
  "s_mul_i32 s89, s89, 40960\n\t"
  "s_add_u32 s89, s89, s45\n\t"
  "s_add_u32 s90, s88, s52\n\t"
  "s_lshr_b32 s96, s90, 21\n\t"
  "s_bitcmp1_b64 s[56:57], s96\n\t"
  "s_cselect_b32 s96, 0x2000, 0\n\t"
  "s_add_u32 s90, s90, s96\n\t"
  "s_add_u32 m0, s89, 0\n\t"
  "s_nop 0\n\t"
  "buffer_load_dwordx4 v0, s[40:43], s90 offen sc1 lds\n\t"
  "buffer_load_dwordx4 v0, s[40:43], s90 offen offset:1024 sc1 lds\n\t"
  "s_add_u32 s90, s88, 33554432\n\t"
  "s_lshr_b32 s96, s90, 21\n\t"
  "s_bitcmp1_b64 s[56:57], s96\n\t"
  "s_cselect_b32 s96, 0x2000, 0\n\t"
  "s_add_u32 s90, s90, s96\n\t"
  "s_add_u32 m0, s89, 4096\n\t"
  "s_nop 0\n\t"
  "buffer_load_dwordx4 v0, s[40:43], s90 offen sc1 lds\n\t"
  "buffer_load_dwordx4 v0, s[40:43], s90 offen offset:1024 sc1 lds\n\t"
  "s_add_u32 s90, s88, 67108864\n\t"
  "s_lshr_b32 s96, s90, 21\n\t"
  "s_bitcmp1_b64 s[56:57], s96\n\t"
  "s_cselect_b32 s96, 0x2000, 0\n\t"
  "s_add_u32 s90, s90, s96\n\t"
  "s_add_u32 m0, s89, 12288\n\t"
  "s_nop 0\n\t"
  "buffer_load_dwordx4 v0, s[40:43], s90 offen sc1 lds\n\t"
  "buffer_load_dwordx4 v0, s[40:43], s90 offen offset:1024 sc1 lds\n\t"
  "s_add_u32 s90, s88, 33558528\n\t"
  "s_lshr_b32 s96, s90, 21\n\t"
  "s_bitcmp1_b64 s[56:57], s96\n\t"
  "s_cselect_b32 s96, 0x2000, 0\n\t"
  "s_add_u32 s90, s90, s96\n\t"
  "s_or_b32 s90, s90, s95\n\t"
  "s_add_u32 m0, s89, 8192\n\t"
  "s_nop 0\n\t"
  "buffer_load_dwordx4 v0, s[40:43], s90 offen sc1 lds\n\t"
  "buffer_load_dwordx4 v0, s[40:43], s90 offen offset:1024 sc1 lds\n\t"
  "s_add_u32 s90, s88, 67112960\n\t"
  "s_lshr_b32 s96, s90, 21\n\t"
  "s_bitcmp1_b64 s[56:57], s96\n\t"
  "s_cselect_b32 s96, 0x2000, 0\n\t"
  "s_add_u32 s90, s90, s96\n\t"
  "s_or_b32 s90, s90, s95\n\t"
  "s_add_u32 m0, s89, 16384\n\t"
  "s_nop 0\n\t"
  "buffer_load_dwordx4 v0, s[40:43], s90 offen sc1 lds\n\t"
  "buffer_load_dwordx4 v0, s[40:43], s90 offen offset:1024 sc1 lds\n\t"
  "s_mov_b32 s92, 1\n\t"
  "s_min_u32 s91, s92, 63\n\t"
  "v_cmp_ge_i32 vcc, s91, v12\n\t"
  "s_cbranch_vccz .Latt_qbok\n\t"
  "s_mov_b32 s80, 0\n\t"
  ".Latt_qb:\n\t"
  "buffer_load_dword v12, v4, s[40:43], s54 offen sc1\n\t"
  "s_waitcnt vmcnt(0)\n\t"
  "v_cmp_ge_i32 vcc, s91, v12\n\t"
  "s_cbranch_vccz .Latt_qbok\n\t"
  "s_sleep 3\n\t"
  "s_add_u32 s80, s80, 1\n\t"
  "s_and_b32 s81, s80, 255\n\t"
  "s_cmp_lg_u32 s81, 0\n\t"
  "s_cbranch_scc1 .Latt_qb\n\t"
  "buffer_load_dword v9, off, s[40:43], s49 sc1\n\t"
  "s_waitcnt vmcnt(0)\n\t"
  "v_readfirstlane_b32 s81, v9\n\t"
  "s_nop 1\n\t"
  "s_cmp_lg_u32 s81, 0\n\t"
  "s_cbranch_scc1 .Latt_end\n\t"
  "s_cmp_lt_u32 s80, 0x80000\n\t"
  "s_cbranch_scc1 .Latt_qb\n\t"
  "v_mov_b32 v9, 1\n\t"
  "buffer_store_dword v9, off, s[40:43], s49 sc1\n\t"
  "s_branch .Latt_end\n\t"
  ".Latt_qbok:\n\t"
  "s_lshl_b32 s88, s92, 19\n\t"
  "s_add_u32 s88, s88, s44\n\t"
  "s_cmp_lt_u32 s92, 64\n\t"
  "s_cselect_b32 s88, s88, 0xe0000000\n\t"
  "s_and_b32 s89, s92, 1\n\t"
  "s_mul_i32 s89, s89, 40960\n\t"
  "s_add_u32 s89, s89, s45\n\t"
  "s_add_u32 s90, s88, s52\n\t"
  "s_lshr_b32 s96, s90, 21\n\t"
  "s_bitcmp1_b64 s[56:57], s96\n\t"
  "s_cselect_b32 s96, 0x2000, 0\n\t"
  "s_add_u32 s90, s90, s96\n\t"
  "s_add_u32 m0, s89, 0\n\t"
  "s_nop 0\n\t"
  "buffer_load_dwordx4 v0, s[40:43], s90 offen sc1 lds\n\t"
  "buffer_load_dwordx4 v0, s[40:43], s90 offen offset:1024 sc1 lds\n\t"
  "s_add_u32 s90, s88, 33554432\n\t"
  "s_lshr_b32 s96, s90, 21\n\t"
  "s_bitcmp1_b64 s[56:57], s96\n\t"
  "s_cselect_b32 s96, 0x2000, 0\n\t"
  "s_add_u32 s90, s90, s96\n\t"
  "s_add_u32 m0, s89, 4096\n\t"
  "s_nop 0\n\t"
  "buffer_load_dwordx4 v0, s[40:43], s90 offen sc1 lds\n\t"
  "buffer_load_dwordx4 v0, s[40:43], s90 offen offset:1024 sc1 lds\n\t"
  "s_add_u32 s90, s88, 67108864\n\t"
  "s_lshr_b32 s96, s90, 21\n\t"
  "s_bitcmp1_b64 s[56:57], s96\n\t"
  "s_cselect_b32 s96, 0x2000, 0\n\t"
  "s_add_u32 s90, s90, s96\n\t"
  "s_add_u32 m0, s89, 12288\n\t"
  "s_nop 0\n\t"
  "buffer_load_dwordx4 v0, s[40:43], s90 offen sc1 lds\n\t"
  "buffer_load_dwordx4 v0, s[40:43], s90 offen offset:1024 sc1 lds\n\t"
  "s_add_u32 s90, s88, 33558528\n\t"
  "s_lshr_b32 s96, s90, 21\n\t"
  "s_bitcmp1_b64 s[56:57], s96\n\t"
  "s_cselect_b32 s96, 0x2000, 0\n\t"
  "s_add_u32 s90, s90, s96\n\t"
  "s_or_b32 s90, s90, s95\n\t"
  "s_add_u32 m0, s89, 8192\n\t"
  "s_nop 0\n\t"
  "buffer_load_dwordx4 v0, s[40:43], s90 offen sc1 lds\n\t"
  "buffer_load_dwordx4 v0, s[40:43], s90 offen offset:1024 sc1 lds\n\t"
  "s_add_u32 s90, s88, 67112960\n\t"
  "s_lshr_b32 s96, s90, 21\n\t"
  "s_bitcmp1_b64 s[56:57], s96\n\t"
  "s_cselect_b32 s96, 0x2000, 0\n\t"
  "s_add_u32 s90, s90, s96\n\t"
  "s_or_b32 s90, s90, s95\n\t"
  "s_add_u32 m0, s89, 16384\n\t"
  "s_nop 0\n\t"
  "buffer_load_dwordx4 v0, s[40:43], s90 offen sc1 lds\n\t"
  "buffer_load_dwordx4 v0, s[40:43], s90 offen offset:1024 sc1 lds\n\t"
  "s_waitcnt vmcnt(0)\n\t"
  "s_barrier\n\t"
  "s_and_b32 s81, s83, 1\n\t"
  "s_mul_i32 s81, s81, 40960\n\t"
  "v_add_u32 v14, s81, v1\n\t"
  "v_add_u32 v15, s81, v5\n\t"
  "ds_read_b128 v[16:19], v14 offset:0\n\t"
  "ds_read_b128 v[32:35], v14 offset:4096\n\t"
  "ds_read_b128 v[48:51], v14 offset:8192\n\t"
  "ds_read_b128 v[20:23], v14 offset:1024\n\t"
  "ds_read_b128 v[36:39], v14 offset:5120\n\t"
  "ds_read_b128 v[52:55], v14 offset:9216\n\t"
  "ds_read_b128 v[24:27], v14 offset:2048\n\t"
  "ds_read_b128 v[40:43], v14 offset:6144\n\t"
  "ds_read_b128 v[56:59], v14 offset:10240\n\t"
  "ds_read_b128 v[28:31], v14 offset:3072\n\t"
  "ds_read_b128 v[44:47], v14 offset:7168\n\t"
  "ds_read_b128 v[60:63], v14 offset:11264\n\t"
  "ds_read_b64_tr_b16 v[64:65], v15 offset:0\n\t"
  "ds_read_b64_tr_b16 v[66:67], v15 offset:4096\n\t"
  "ds_read_b64_tr_b16 v[68:69], v15 offset:8\n\t"
  "ds_read_b64_tr_b16 v[70:71], v15 offset:4104\n\t"
  "ds_read_b64_tr_b16 v[72:73], v15 offset:1024\n\t"
  "ds_read_b64_tr_b16 v[74:75], v15 offset:5120\n\t"
  "ds_read_b64_tr_b16 v[76:77], v15 offset:1032\n\t"
  "ds_read_b64_tr_b16 v[78:79], v15 offset:5128\n\t"
  "s_waitcnt lgkmcnt(15)\n\t"
  "v_mfma_f32_16x16x32_bf16 v[104:107], v[32:35], v[16:19], 0\n\t"
  "v_mfma_f32_16x16x32_bf16 v[108:111], v[48:51], v[16:19], 0\n\t"
  "v_mfma_f32_16x16x32_bf16 v[104:107], v[36:39], v[20:23], v[104:107]\n\t"
  "s_waitcnt lgkmcnt(14)\n\t"
  "v_mfma_f32_16x16x32_bf16 v[108:111], v[52:55], v[20:23], v[108:111]\n\t"
  "s_waitcnt lgkmcnt(12)\n\t"
  "v_mfma_f32_16x16x32_bf16 v[104:107], v[40:43], v[24:27], v[104:107]\n\t"
  "s_waitcnt lgkmcnt(11)\n\t"
  "v_mfma_f32_16x16x32_bf16 v[108:111], v[56:59], v[24:27], v[108:111]\n\t"
  "s_waitcnt lgkmcnt(9)\n\t"
  "v_mfma_f32_16x16x32_bf16 v[104:107], v[44:47], v[28:31], v[104:107]\n\t"
  "s_waitcnt lgkmcnt(8)\n\t"
  "v_mfma_f32_16x16x32_bf16 v[108:111], v[60:63], v[28:31], v[108:111]\n\t"
  "s_nop 4\n\t"
  "v_cndmask_b32 v104, v104, v123, s[60:61]\n\t"
  "v_cndmask_b32 v105, v105, v123, s[62:63]\n\t"
  "v_cndmask_b32 v106, v106, v123, s[64:65]\n\t"
  "v_cndmask_b32 v107, v107, v123, s[66:67]\n\t"
  "v_cndmask_b32 v108, v108, v123, s[68:69]\n\t"
  "v_cndmask_b32 v109, v109, v123, s[70:71]\n\t"
  "v_cndmask_b32 v110, v110, v123, s[72:73]\n\t"
  "v_cndmask_b32 v111, v111, v123, s[74:75]\n\t"
  "v_max3_f32 v116, v104, v105, v106\n\t"
  "v_max3_f32 v117, v107, v108, v109\n\t"
  "v_max3_f32 v116, v116, v110, v111\n\t"
  "v_max_f32 v116, v116, v117\n\t"
  "s_waitcnt lgkmcnt(0)\n\t"
  "s_mov_b32 s80, 0\n\t"
  ".Latt_i0:\n\t"
  "buffer_load_dword v13, v3, s[40:43], s54 offen sc1\n\t"
  "s_waitcnt vmcnt(0)\n\t"
  "v_cmp_ge_i32 vcc, s83, v13\n\t"
  "s_cbranch_vccz .Latt_i0ok\n\t"
  "s_sleep 3\n\t"
  "s_add_u32 s80, s80, 1\n\t"
  "s_and_b32 s81, s80, 255\n\t"
  "s_cmp_lg_u32 s81, 0\n\t"
  "s_cbranch_scc1 .Latt_i0\n\t"
  "buffer_load_dword v9, off, s[40:43], s49 sc1\n\t"
  "s_waitcnt vmcnt(0)\n\t"
  "v_readfirstlane_b32 s81, v9\n\t"
  "s_nop 1\n\t"
  "s_cmp_lg_u32 s81, 0\n\t"
  "s_cbranch_scc1 .Latt_end\n\t"
  "s_cmp_lt_u32 s80, 0x80000\n\t"
  "s_cbranch_scc1 .Latt_i0\n\t"
  "v_mov_b32 v9, 1\n\t"
  "buffer_store_dword v9, off, s[40:43], s49 sc1\n\t"
  "s_branch .Latt_end\n\t"
  ".Latt_i0ok:\n\t"
  "s_lshl_b32 s81, s83, 18\n\t"
  "s_add_u32 s81, s81, s48\n\t"
  "s_lshr_b32 s82, s81, 21\n\t"
  "s_bitcmp1_b64 s[56:57], s82\n\t"
  "s_cselect_b32 s82, 0x2000, 0\n\t"
  "s_add_u32 s81, s81, s82\n\t"
  "s_mov_b32 m0, s50\n\t"
  "s_nop 0\n\t"
  "buffer_load_dwordx4 v0, s[40:43], s81 offen sc1 lds\n\t"
  "buffer_load_dwordx4 v0, s[40:43], s81 offen offset:1024 sc1 lds\n\t"
  "buffer_load_dwordx2 v[80:81], v7, s[40:43], s81 offen offset:2048 sc1\n\t"
  "buffer_load_dwordx2 v[82:83], v7, s[40:43], s81 offen offset:2112 sc1\n\t"
  "buffer_load_dwordx2 v[84:85], v7, s[40:43], s81 offen offset:2560 sc1\n\t"
  "buffer_load_dwordx2 v[86:87], v7, s[40:43], s81 offen offset:2624 sc1\n\t"
  "s_waitcnt vmcnt(0)\n\t"
  ".Latt_loop:\n\t"
  ".Latt_go:\n\t"
  "ds_read_b64_tr_b16 v[88:89], v6 offset:0\n\t"
  "ds_read_b64_tr_b16 v[90:91], v6 offset:64\n\t"
  "ds_read_b64_tr_b16 v[92:93], v6 offset:512\n\t"
  "ds_read_b64_tr_b16 v[94:95], v6 offset:576\n\t"
  "ds_read_b64_tr_b16 v[96:97], v6 offset:1024\n\t"
  "ds_read_b64_tr_b16 v[98:99], v6 offset:1088\n\t"
  "ds_read_b64_tr_b16 v[100:101], v6 offset:1536\n\t"
  "ds_read_b64_tr_b16 v[102:103], v6 offset:1600\n\t"
  "s_and_b32 s81, s83, 1\n\t"
  "s_lshl_b32 s81, s81, 13\n\t"
  "v_add_u32 v122, s81, v8\n\t"
  "v_add_u32 v14, s53, v122\n\t"
  "s_waitcnt lgkmcnt(6)\n\t"
  "v_mfma_f32_16x16x32_bf16 v[112:115], v[88:91], v[16:19], 0\n\t"
  "s_waitcnt lgkmcnt(4)\n\t"
  "v_mfma_f32_16x16x32_bf16 v[112:115], v[92:95], v[20:23], v[112:115]\n\t"
  "s_waitcnt lgkmcnt(2)\n\t"
  "v_mfma_f32_16x16x32_bf16 v[112:115], v[96:99], v[24:27], v[112:115]\n\t"
  "s_waitcnt lgkmcnt(0)\n\t"
  "v_mfma_f32_16x16x32_bf16 v[112:115], v[100:103], v[28:31], v[112:115]\n\t"
  "s_add_u32 s93, s83, 1\n\t"
  "s_add_u32 s92, s83, 2\n\t"
  "s_lshl_b32 s94, s83, 21\n\t"
  "s_add_u32 s94, s94, s46\n\t"
  "s_bitcmp1_b64 s[58:59], s83\n\t"
  "s_cselect_b32 s81, 0x2000, 0\n\t"
  "s_add_u32 s94, s94, s81\n\t"
  "s_nop 0\n\t"
  "v_max3_f32 v116, v116, v112, v113\n\t"
  "v_max3_f32 v116, v116, v114, v115\n\t"
  "v_mov_b32 v117, v116\n\t"
  "s_nop 1\n\t"
  "v_permlane16_swap_b32 v116, v117\n\t"
  "v_max_f32 v116, v116, v117\n\t"
  "v_mov_b32 v117, v116\n\t"
  "s_nop 1\n\t"
  "v_permlane32_swap_b32 v116, v117\n\t"
  "v_max_f32 v116, v116, v117\n\t"
  "v_mul_f32 v118, s55, v116\n\t"
  "v_fma_f32 v119, s55, v116, 1.0\n\t"
  "v_fma_f32 v104, v104, s55, -v118\n\t"
  "v_fma_f32 v105, v105, s55, -v118\n\t"
  "v_fma_f32 v106, v106, s55, -v118\n\t"
  "v_fma_f32 v107, v107, s55, -v118\n\t"
  "v_fma_f32 v108, v108, s55, -v118\n\t"
  "v_fma_f32 v109, v109, s55, -v118\n\t"
  "v_fma_f32 v110, v110, s55, -v118\n\t"
  "v_fma_f32 v111, v111, s55, -v118\n\t"
  "v_fma_f32 v112, v112, s55, -v119\n\t"
  "v_fma_f32 v113, v113, s55, -v119\n\t"
  "v_fma_f32 v114, v114, s55, -v119\n\t"
  "v_fma_f32 v115, v115, s55, -v119\n\t"
  "v_exp_f32 v104, v104\n\t"
  "v_exp_f32 v105, v105\n\t"
  "v_exp_f32 v106, v106\n\t"
  "v_exp_f32 v107, v107\n\t"
  "v_exp_f32 v108, v108\n\t"
  "v_exp_f32 v109, v109\n\t"
  "v_exp_f32 v110, v110\n\t"
  "v_exp_f32 v111, v111\n\t"
  "v_exp_f32 v112, v112\n\t"
  "v_exp_f32 v113, v113\n\t"
  "v_exp_f32 v114, v114\n\t"
  "v_exp_f32 v115, v115\n\t"
  "v_cvt_pk_bf16_f32 v124, v104, v105\n\t"
  "v_cvt_pk_bf16_f32 v125, v106, v107\n\t"
  "v_cvt_pk_bf16_f32 v126, v108, v109\n\t"
  "v_cvt_pk_bf16_f32 v127, v110, v111\n\t"
  "v_cvt_pk_bf16_f32 v128, v112, v113\n\t"
  "v_cvt_pk_bf16_f32 v129, v114, v115\n\t"
  "v_mfma_f32_16x16x32_bf16 v[132:135], v[64:67], v[124:127], 0\n\t"
  "v_add_f32 v104, v104, v110\n\t"
  "v_add_f32 v105, v105, v111\n\t"
  "v_mfma_f32_16x16x32_bf16 v[136:139], v[68:71], v[124:127], 0\n\t"
  "v_add_f32 v106, v106, v112\n\t"
  "v_add_f32 v107, v107, v113\n\t"
  "v_mfma_f32_16x16x32_bf16 v[140:143], v[72:75], v[124:127], 0\n\t"
  "v_add_f32 v108, v108, v114\n\t"
  "v_add_f32 v109, v109, v115\n\t"
  "v_mfma_f32_16x16x32_bf16 v[144:147], v[76:79], v[124:127], 0\n\t"
  "v_add_f32 v104, v104, v107\n\t"
  "v_add_f32 v105, v105, v108\n\t"
  "s_waitcnt vmcnt(1)\n\t"
  "v_mfma_f32_16x16x16_bf16 v[132:135], v[80:81], v[128:129], v[132:135]\n\t"
  "v_add_f32 v106, v106, v109\n\t"
  "v_add_f32 v104, v104, v105\n\t"
  "v_mfma_f32_16x16x16_bf16 v[136:139], v[82:83], v[128:129], v[136:139]\n\t"
  "v_add_f32 v120, v104, v106\n\t"
  "v_mov_b32 v117, v120\n\t"
  "v_mfma_f32_16x16x16_bf16 v[140:143], v[84:85], v[128:129], v[140:143]\n\t"
  "s_nop 0\n\t"
  "v_permlane16_swap_b32 v120, v117\n\t"
  "v_mfma_f32_16x16x16_bf16 v[144:147], v[86:87], v[128:129], v[144:147]\n\t"
  "v_add_f32 v120, v120, v117\n\t"
  "v_mov_b32 v117, v120\n\t"
  "s_nop 1\n\t"
  "v_permlane32_swap_b32 v120, v117\n\t"
  "v_add_f32 v120, v120, v117\n\t"
  "v_rcp_f32 v120, v120\n\t"
  "s_nop 0\n\t"
  "v_pk_mul_f32 v[132:133], v[132:133], v[120:121] op_sel_hi:[1,0]\n\t"
  "v_pk_mul_f32 v[134:135], v[134:135], v[120:121] op_sel_hi:[1,0]\n\t"
  "v_pk_mul_f32 v[136:137], v[136:137], v[120:121] op_sel_hi:[1,0]\n\t"
  "v_pk_mul_f32 v[138:139], v[138:139], v[120:121] op_sel_hi:[1,0]\n\t"
  "v_cvt_pk_bf16_f32 v132, v132, v133\n\t"
  "v_cvt_pk_bf16_f32 v133, v134, v135\n\t"
  "v_cvt_pk_bf16_f32 v134, v136, v137\n\t"
  "v_cvt_pk_bf16_f32 v135, v138, v139\n\t"
  "ds_write_b128 v14, v[132:135] offset:0\n\t"
  "v_pk_mul_f32 v[140:141], v[140:141], v[120:121] op_sel_hi:[1,0]\n\t"
  "v_pk_mul_f32 v[142:143], v[142:143], v[120:121] op_sel_hi:[1,0]\n\t"
  "v_pk_mul_f32 v[144:145], v[144:145], v[120:121] op_sel_hi:[1,0]\n\t"
  "v_pk_mul_f32 v[146:147], v[146:147], v[120:121] op_sel_hi:[1,0]\n\t"
  "v_cvt_pk_bf16_f32 v140, v140, v141\n\t"
  "v_cvt_pk_bf16_f32 v141, v142, v143\n\t"
  "v_cvt_pk_bf16_f32 v142, v144, v145\n\t"
  "v_cvt_pk_bf16_f32 v143, v146, v147\n\t"
  "ds_write_b128 v14, v[140:143] offset:1024\n\t"
  "s_waitcnt lgkmcnt(0)\n\t"
  "s_barrier\n\t"
  "ds_read_b128 v[148:151], v122 offset:0\n\t"
  "ds_read_b128 v[152:155], v122 offset:1024\n\t"
  "ds_read_b128 v[156:159], v122 offset:2048\n\t"
  "ds_read_b128 v[160:163], v122 offset:3072\n\t"
  "ds_read_b128 v[164:167], v122 offset:4096\n\t"
  "ds_read_b128 v[168:171], v122 offset:5120\n\t"
  "ds_read_b128 v[172:175], v122 offset:6144\n\t"
  "ds_read_b128 v[176:179], v122 offset:7168\n\t"
  "s_min_u32 s91, s92, 63\n\t"
  "v_cmp_ge_i32 vcc, s91, v12\n\t"
  "s_cbranch_vccz .Latt_qlok\n\t"
  "s_mov_b32 s80, 0\n\t"
  ".Latt_ql:\n\t"
  "buffer_load_dword v12, v4, s[40:43], s54 offen sc1\n\t"
  "s_waitcnt vmcnt(0)\n\t"
  "v_cmp_ge_i32 vcc, s91, v12\n\t"
  "s_cbranch_vccz .Latt_qlok\n\t"
  "s_sleep 3\n\t"
  "s_add_u32 s80, s80, 1\n\t"
  "s_and_b32 s81, s80, 255\n\t"
  "s_cmp_lg_u32 s81, 0\n\t"
  "s_cbranch_scc1 .Latt_ql\n\t"
  "buffer_load_dword v9, off, s[40:43], s49 sc1\n\t"
  "s_waitcnt vmcnt(0)\n\t"
  "v_readfirstlane_b32 s81, v9\n\t"
  "s_nop 1\n\t"
  "s_cmp_lg_u32 s81, 0\n\t"
  "s_cbranch_scc1 .Latt_end\n\t"
  "s_cmp_lt_u32 s80, 0x80000\n\t"
  "s_cbranch_scc1 .Latt_ql\n\t"
  "v_mov_b32 v9, 1\n\t"
  "buffer_store_dword v9, off, s[40:43], s49 sc1\n\t"
  "s_branch .Latt_end\n\t"
  ".Latt_qlok:\n\t"
  "s_waitcnt lgkmcnt(7)\n\t"
  "v_mfma_f32_16x16x32_bf16 v[180:183], a[0:3], v[148:151], 0\n\t"
  "s_lshl_b32 s88, s92, 19\n\t"
  "s_add_u32 s88, s88, s44\n\t"
  "s_cmp_lt_u32 s92, 64\n\t"
  "s_cselect_b32 s88, s88, 0xe0000000\n\t"
  "s_and_b32 s89, s92, 1\n\t"
  "s_mul_i32 s89, s89, 40960\n\t"
  "s_add_u32 s89, s89, s45\n\t"
  "s_add_u32 s90, s88, s52\n\t"
  "s_lshr_b32 s96, s90, 21\n\t"
  "s_bitcmp1_b64 s[56:57], s96\n\t"
  "s_cselect_b32 s96, 0x2000, 0\n\t"
  "s_add_u32 s90, s90, s96\n\t"
  "s_add_u32 m0, s89, 0\n\t"
  "s_nop 0\n\t"
  "buffer_load_dwordx4 v0, s[40:43], s90 offen sc1 lds\n\t"
  "v_mfma_f32_16x16x32_bf16 v[184:187], a[32:35], v[148:151], 0\n\t"
  "s_and_b32 s81, s93, 1\n\t"
  "s_mul_i32 s81, s81, 40960\n\t"
  "v_add_u32 v14, s81, v1\n\t"
  "v_add_u32 v15, s81, v5\n\t"
  "s_waitcnt lgkmcnt(6)\n\t"
  "v_mfma_f32_16x16x32_bf16 v[180:183], a[4:7], v[152:155], v[180:183]\n\t"
  "ds_read_b128 v[16:19], v14 offset:0\n\t"
  "v_mfma_f32_16x16x32_bf16 v[184:187], a[36:39], v[152:155], v[184:187]\n\t"
  "buffer_load_dwordx4 v0, s[40:43], s90 offen offset:1024 sc1 lds\n\t"
  "s_waitcnt lgkmcnt(6)\n\t"
  "v_mfma_f32_16x16x32_bf16 v[180:183], a[8:11], v[156:159], v[180:183]\n\t"
  "ds_read_b128 v[32:35], v14 offset:4096\n\t"
  "v_mfma_f32_16x16x32_bf16 v[184:187], a[40:43], v[156:159], v[184:187]\n\t"
  "ds_read_b128 v[48:51], v14 offset:8192\n\t"
  "s_waitcnt lgkmcnt(7)\n\t"
  "v_mfma_f32_16x16x32_bf16 v[180:183], a[12:15], v[160:163], v[180:183]\n\t"
  "s_add_u32 s90, s88, 33554432\n\t"
  "s_lshr_b32 s96, s90, 21\n\t"
  "s_bitcmp1_b64 s[56:57], s96\n\t"
  "s_cselect_b32 s96, 0x2000, 0\n\t"
  "s_add_u32 s90, s90, s96\n\t"
  "s_add_u32 m0, s89, 4096\n\t"
  "s_nop 0\n\t"
  "buffer_load_dwordx4 v0, s[40:43], s90 offen sc1 lds\n\t"
  "v_mfma_f32_16x16x32_bf16 v[184:187], a[44:47], v[160:163], v[184:187]\n\t"
  "ds_read_b128 v[20:23], v14 offset:1024\n\t"
  "s_waitcnt lgkmcnt(7)\n\t"
  "v_mfma_f32_16x16x32_bf16 v[180:183], a[16:19], v[164:167], v[180:183]\n\t"
  "ds_read_b128 v[36:39], v14 offset:5120\n\t"
  "v_mfma_f32_16x16x32_bf16 v[184:187], a[48:51], v[164:167], v[184:187]\n\t"
  "buffer_load_dwordx4 v0, s[40:43], s90 offen offset:1024 sc1 lds\n\t"
  "s_waitcnt lgkmcnt(7)\n\t"
  "v_mfma_f32_16x16x32_bf16 v[180:183], a[20:23], v[168:171], v[180:183]\n\t"
  "ds_read_b128 v[52:55], v14 offset:9216\n\t"
  "v_mfma_f32_16x16x32_bf16 v[184:187], a[52:55], v[168:171], v[184:187]\n\t"
  "ds_read_b128 v[24:27], v14 offset:2048\n\t"
  "s_waitcnt lgkmcnt(8)\n\t"
  "v_mfma_f32_16x16x32_bf16 v[180:183], a[24:27], v[172:175], v[180:183]\n\t"
  "s_add_u32 s90, s88, 67108864\n\t"
  "s_lshr_b32 s96, s90, 21\n\t"
  "s_bitcmp1_b64 s[56:57], s96\n\t"
  "s_cselect_b32 s96, 0x2000, 0\n\t"
  "s_add_u32 s90, s90, s96\n\t"
  "s_add_u32 m0, s89, 12288\n\t"
  "s_nop 0\n\t"
  "buffer_load_dwordx4 v0, s[40:43], s90 offen sc1 lds\n\t"
  "v_mfma_f32_16x16x32_bf16 v[184:187], a[56:59], v[172:175], v[184:187]\n\t"
  "ds_read_b128 v[40:43], v14 offset:6144\n\t"
  "s_waitcnt lgkmcnt(8)\n\t"
  "v_mfma_f32_16x16x32_bf16 v[180:183], a[28:31], v[176:179], v[180:183]\n\t"
  "ds_read_b128 v[56:59], v14 offset:10240\n\t"
  "v_mfma_f32_16x16x32_bf16 v[184:187], a[60:63], v[176:179], v[184:187]\n\t"
  "buffer_load_dwordx4 v0, s[40:43], s90 offen offset:1024 sc1 lds\n\t"
  "v_mfma_f32_16x16x32_bf16 v[188:191], a[64:67], v[148:151], 0\n\t"
  "ds_read_b128 v[28:31], v14 offset:3072\n\t"
  "v_mfma_f32_16x16x32_bf16 v[192:195], a[96:99], v[148:151], 0\n\t"
  "ds_read_b128 v[44:47], v14 offset:7168\n\t"
  "v_mfma_f32_16x16x32_bf16 v[188:191], a[68:71], v[152:155], v[188:191]\n\t"
  "v_cvt_pk_bf16_f32 v196, v180, v181\n\t"
  "v_cvt_pk_bf16_f32 v197, v182, v183\n\t"
  "v_mfma_f32_16x16x32_bf16 v[192:195], a[100:103], v[152:155], v[192:195]\n\t"
  "v_cvt_pk_bf16_f32 v198, v184, v185\n\t"
  "v_cvt_pk_bf16_f32 v199, v186, v187\n\t"
  "v_mfma_f32_16x16x32_bf16 v[188:191], a[72:75], v[156:159], v[188:191]\n\t"
  "buffer_store_dwordx4 v[196:199], v2, s[40:43], s94 offen offset:0 sc1\n\t"
  "v_mfma_f32_16x16x32_bf16 v[192:195], a[104:107], v[156:159], v[192:195]\n\t"
  "s_add_u32 s90, s88, 33558528\n\t"
  "s_lshr_b32 s96, s90, 21\n\t"
  "s_bitcmp1_b64 s[56:57], s96\n\t"
  "s_cselect_b32 s96, 0x2000, 0\n\t"
  "s_add_u32 s90, s90, s96\n\t"
  "s_or_b32 s90, s90, s95\n\t"
  "s_add_u32 m0, s89, 8192\n\t"
  "s_nop 0\n\t"
  "buffer_load_dwordx4 v0, s[40:43], s90 offen sc1 lds\n\t"
  "v_mfma_f32_16x16x32_bf16 v[188:191], a[76:79], v[160:163], v[188:191]\n\t"
  "ds_read_b128 v[60:63], v14 offset:11264\n\t"
  "v_mfma_f32_16x16x32_bf16 v[192:195], a[108:111], v[160:163], v[192:195]\n\t"
  "ds_read_b64_tr_b16 v[64:65], v15 offset:0\n\t"
  "v_mfma_f32_16x16x32_bf16 v[188:191], a[80:83], v[164:167], v[188:191]\n\t"
  "buffer_load_dwordx4 v0, s[40:43], s90 offen offset:1024 sc1 lds\n\t"
  "v_mfma_f32_16x16x32_bf16 v[192:195], a[112:115], v[164:167], v[192:195]\n\t"
  "ds_read_b64_tr_b16 v[66:67], v15 offset:4096\n\t"
  "v_mfma_f32_16x16x32_bf16 v[188:191], a[84:87], v[168:171], v[188:191]\n\t"
  "ds_read_b64_tr_b16 v[68:69], v15 offset:8\n\t"
  "v_mfma_f32_16x16x32_bf16 v[192:195], a[116:119], v[168:171], v[192:195]\n\t"
  "s_add_u32 s90, s88, 67112960\n\t"
  "s_lshr_b32 s96, s90, 21\n\t"
  "s_bitcmp1_b64 s[56:57], s96\n\t"
  "s_cselect_b32 s96, 0x2000, 0\n\t"
  "s_add_u32 s90, s90, s96\n\t"
  "s_or_b32 s90, s90, s95\n\t"
  "s_add_u32 m0, s89, 16384\n\t"
  "s_nop 0\n\t"
  "buffer_load_dwordx4 v0, s[40:43], s90 offen sc1 lds\n\t"
  "v_mfma_f32_16x16x32_bf16 v[188:191], a[88:91], v[172:175], v[188:191]\n\t"
  "ds_read_b64_tr_b16 v[70:71], v15 offset:4104\n\t"
  "v_mfma_f32_16x16x32_bf16 v[192:195], a[120:123], v[172:175], v[192:195]\n\t"
  "ds_read_b64_tr_b16 v[72:73], v15 offset:1024\n\t"
  "v_mfma_f32_16x16x32_bf16 v[188:191], a[92:95], v[176:179], v[188:191]\n\t"
  "buffer_load_dwordx4 v0, s[40:43], s90 offen offset:1024 sc1 lds\n\t"
  "v_mfma_f32_16x16x32_bf16 v[192:195], a[124:127], v[176:179], v[192:195]\n\t"
  "ds_read_b64_tr_b16 v[74:75], v15 offset:5120\n\t"
  "v_mfma_f32_16x16x32_bf16 v[180:183], a[128:131], v[148:151], 0\n\t"
  "ds_read_b64_tr_b16 v[76:77], v15 offset:1032\n\t"
  "v_mfma_f32_16x16x32_bf16 v[184:187], a[160:163], v[148:151], 0\n\t"
  "ds_read_b64_tr_b16 v[78:79], v15 offset:5128\n\t"
  "v_mfma_f32_16x16x32_bf16 v[180:183], a[132:135], v[152:155], v[180:183]\n\t"
  "v_cvt_pk_bf16_f32 v200, v188, v189\n\t"
  "v_cvt_pk_bf16_f32 v201, v190, v191\n\t"
  "v_mfma_f32_16x16x32_bf16 v[184:187], a[164:167], v[152:155], v[184:187]\n\t"
  "v_cvt_pk_bf16_f32 v202, v192, v193\n\t"
  "v_cvt_pk_bf16_f32 v203, v194, v195\n\t"
  "v_mfma_f32_16x16x32_bf16 v[180:183], a[136:139], v[156:159], v[180:183]\n\t"
  "buffer_store_dwordx4 v[200:203], v2, s[40:43], s94 offen offset:256 sc1\n\t"
  "v_mfma_f32_16x16x32_bf16 v[184:187], a[168:171], v[156:159], v[184:187]\n\t"
  "v_mfma_f32_16x16x32_bf16 v[180:183], a[140:143], v[160:163], v[180:183]\n\t"
  "buffer_load_dword v13, v3, s[40:43], s54 offen sc1\n\t"
  "v_mfma_f32_16x16x32_bf16 v[184:187], a[172:175], v[160:163], v[184:187]\n\t"
  "v_mfma_f32_16x16x32_bf16 v[180:183], a[144:147], v[164:167], v[180:183]\n\t"
  "v_mfma_f32_16x16x32_bf16 v[184:187], a[176:179], v[164:167], v[184:187]\n\t"
  "v_mfma_f32_16x16x32_bf16 v[180:183], a[148:151], v[168:171], v[180:183]\n\t"
  "v_mfma_f32_16x16x32_bf16 v[184:187], a[180:183], v[168:171], v[184:187]\n\t"
  "v_mfma_f32_16x16x32_bf16 v[180:183], a[152:155], v[172:175], v[180:183]\n\t"
  "v_mfma_f32_16x16x32_bf16 v[184:187], a[184:187], v[172:175], v[184:187]\n\t"
  "v_mfma_f32_16x16x32_bf16 v[180:183], a[156:159], v[176:179], v[180:183]\n\t"
  "v_mfma_f32_16x16x32_bf16 v[184:187], a[188:191], v[176:179], v[184:187]\n\t"
  "v_mfma_f32_16x16x32_bf16 v[188:191], a[192:195], v[148:151], 0\n\t"
  "v_mfma_f32_16x16x32_bf16 v[192:195], a[224:227], v[148:151], 0\n\t"
  "v_mfma_f32_16x16x32_bf16 v[188:191], a[196:199], v[152:155], v[188:191]\n\t"
  "v_cvt_pk_bf16_f32 v196, v180, v181\n\t"
  "v_cvt_pk_bf16_f32 v197, v182, v183\n\t"
  "v_mfma_f32_16x16x32_bf16 v[192:195], a[228:231], v[152:155], v[192:195]\n\t"
  "v_cvt_pk_bf16_f32 v198, v184, v185\n\t"
  "v_cvt_pk_bf16_f32 v199, v186, v187\n\t"
  "v_mfma_f32_16x16x32_bf16 v[188:191], a[200:203], v[156:159], v[188:191]\n\t"
  "buffer_store_dwordx4 v[196:199], v2, s[40:43], s94 offen offset:512 sc1\n\t"
  "v_mfma_f32_16x16x32_bf16 v[192:195], a[232:235], v[156:159], v[192:195]\n\t"
  "v_mfma_f32_16x16x32_bf16 v[188:191], a[204:207], v[160:163], v[188:191]\n\t"
  "v_mfma_f32_16x16x32_bf16 v[192:195], a[236:239], v[160:163], v[192:195]\n\t"
  "v_mfma_f32_16x16x32_bf16 v[188:191], a[208:211], v[164:167], v[188:191]\n\t"
  "v_mfma_f32_16x16x32_bf16 v[192:195], a[240:243], v[164:167], v[192:195]\n\t"
  "v_mfma_f32_16x16x32_bf16 v[188:191], a[212:215], v[168:171], v[188:191]\n\t"
  "v_mfma_f32_16x16x32_bf16 v[192:195], a[244:247], v[168:171], v[192:195]\n\t"
  "v_mfma_f32_16x16x32_bf16 v[188:191], a[216:219], v[172:175], v[188:191]\n\t"
  "v_mfma_f32_16x16x32_bf16 v[192:195], a[248:251], v[172:175], v[192:195]\n\t"
  "v_mfma_f32_16x16x32_bf16 v[188:191], a[220:223], v[176:179], v[188:191]\n\t"
  "v_mfma_f32_16x16x32_bf16 v[192:195], a[252:255], v[176:179], v[192:195]\n\t"
  "s_nop 4\n\t"
  "v_cvt_pk_bf16_f32 v200, v188, v189\n\t"
  "v_cvt_pk_bf16_f32 v201, v190, v191\n\t"
  "s_nop 0\n\t"
  "v_cvt_pk_bf16_f32 v202, v192, v193\n\t"
  "v_cvt_pk_bf16_f32 v203, v194, v195\n\t"
  "buffer_store_dwordx4 v[200:203], v2, s[40:43], s94 offen offset:768 sc1\n\t"
  ".Latt_nx:\n\t"
  "s_cmp_eq_u32 s83, 63\n\t"
  "s_cbranch_scc1 .Latt_fin\n\t"
  "s_waitcnt vmcnt(2)\n\t"
  "v_cmp_ge_i32 vcc, s93, v13\n\t"
  "s_mov_b32 s83, s93\n\t"
  "s_cbranch_vccnz .Latt_wait\n\t"
  "s_lshl_b32 s81, s83, 18\n\t"
  "s_add_u32 s81, s81, s48\n\t"
  "s_lshr_b32 s82, s81, 21\n\t"
  "s_bitcmp1_b64 s[56:57], s82\n\t"
  "s_cselect_b32 s82, 0x2000, 0\n\t"
  "s_add_u32 s81, s81, s82\n\t"
  "s_mov_b32 m0, s50\n\t"
  "s_nop 0\n\t"
  "buffer_load_dwordx4 v0, s[40:43], s81 offen sc1 lds\n\t"
  "buffer_load_dwordx4 v0, s[40:43], s81 offen offset:1024 sc1 lds\n\t"
  "buffer_load_dwordx2 v[80:81], v7, s[40:43], s81 offen offset:2048 sc1\n\t"
  "buffer_load_dwordx2 v[82:83], v7, s[40:43], s81 offen offset:2112 sc1\n\t"
  "buffer_load_dwordx2 v[84:85], v7, s[40:43], s81 offen offset:2560 sc1\n\t"
  "buffer_load_dwordx2 v[86:87], v7, s[40:43], s81 offen offset:2624 sc1\n\t"
  "s_waitcnt lgkmcnt(15)\n\t"
  "v_mfma_f32_16x16x32_bf16 v[104:107], v[32:35], v[16:19], 0\n\t"
  "v_mfma_f32_16x16x32_bf16 v[108:111], v[48:51], v[16:19], 0\n\t"
  "v_mfma_f32_16x16x32_bf16 v[104:107], v[36:39], v[20:23], v[104:107]\n\t"
  "s_waitcnt lgkmcnt(14)\n\t"
  "v_mfma_f32_16x16x32_bf16 v[108:111], v[52:55], v[20:23], v[108:111]\n\t"
  "s_waitcnt lgkmcnt(12)\n\t"
  "v_mfma_f32_16x16x32_bf16 v[104:107], v[40:43], v[24:27], v[104:107]\n\t"
  "s_waitcnt lgkmcnt(11)\n\t"
  "v_mfma_f32_16x16x32_bf16 v[108:111], v[56:59], v[24:27], v[108:111]\n\t"
  "s_waitcnt lgkmcnt(9)\n\t"
  "v_mfma_f32_16x16x32_bf16 v[104:107], v[44:47], v[28:31], v[104:107]\n\t"
  "s_waitcnt lgkmcnt(8)\n\t"
  "v_mfma_f32_16x16x32_bf16 v[108:111], v[60:63], v[28:31], v[108:111]\n\t"
  "s_nop 4\n\t"
  "v_cndmask_b32 v104, v104, v123, s[60:61]\n\t"
  "v_cndmask_b32 v105, v105, v123, s[62:63]\n\t"
  "v_cndmask_b32 v106, v106, v123, s[64:65]\n\t"
  "v_cndmask_b32 v107, v107, v123, s[66:67]\n\t"
  "v_cndmask_b32 v108, v108, v123, s[68:69]\n\t"
  "v_cndmask_b32 v109, v109, v123, s[70:71]\n\t"
  "v_cndmask_b32 v110, v110, v123, s[72:73]\n\t"
  "v_cndmask_b32 v111, v111, v123, s[74:75]\n\t"
  "v_max3_f32 v116, v104, v105, v106\n\t"
  "v_max3_f32 v117, v107, v108, v109\n\t"
  "v_max3_f32 v116, v116, v110, v111\n\t"
  "v_max_f32 v116, v116, v117\n\t"
  "s_waitcnt vmcnt(6) lgkmcnt(0)\n\t"
  "v_mov_b32 v9, s83\n\t"
  "s_mov_b64 exec, 1\n\t"
  "buffer_store_dword v9, off, s[40:43], s47 sc1\n\t"
  "s_mov_b64 exec, -1\n\t"
  "s_waitcnt vmcnt(5)\n\t"
  "s_branch .Latt_loop\n\t"
  ".Latt_wait:\n\t"
  "s_waitcnt lgkmcnt(15)\n\t"
  "v_mfma_f32_16x16x32_bf16 v[104:107], v[32:35], v[16:19], 0\n\t"
  "v_mfma_f32_16x16x32_bf16 v[108:111], v[48:51], v[16:19], 0\n\t"
  "v_mfma_f32_16x16x32_bf16 v[104:107], v[36:39], v[20:23], v[104:107]\n\t"
  "s_waitcnt lgkmcnt(14)\n\t"
  "v_mfma_f32_16x16x32_bf16 v[108:111], v[52:55], v[20:23], v[108:111]\n\t"
  "s_waitcnt lgkmcnt(12)\n\t"
  "v_mfma_f32_16x16x32_bf16 v[104:107], v[40:43], v[24:27], v[104:107]\n\t"
  "s_waitcnt lgkmcnt(11)\n\t"
  "v_mfma_f32_16x16x32_bf16 v[108:111], v[56:59], v[24:27], v[108:111]\n\t"
  "s_waitcnt lgkmcnt(9)\n\t"
  "v_mfma_f32_16x16x32_bf16 v[104:107], v[44:47], v[28:31], v[104:107]\n\t"
  "s_waitcnt lgkmcnt(8)\n\t"
  "v_mfma_f32_16x16x32_bf16 v[108:111], v[60:63], v[28:31], v[108:111]\n\t"
  "s_nop 4\n\t"
  "v_cndmask_b32 v104, v104, v123, s[60:61]\n\t"
  "v_cndmask_b32 v105, v105, v123, s[62:63]\n\t"
  "v_cndmask_b32 v106, v106, v123, s[64:65]\n\t"
  "v_cndmask_b32 v107, v107, v123, s[66:67]\n\t"
  "v_cndmask_b32 v108, v108, v123, s[68:69]\n\t"
  "v_cndmask_b32 v109, v109, v123, s[70:71]\n\t"
  "v_cndmask_b32 v110, v110, v123, s[72:73]\n\t"
  "v_cndmask_b32 v111, v111, v123, s[74:75]\n\t"
  "v_max3_f32 v116, v104, v105, v106\n\t"
  "v_max3_f32 v117, v107, v108, v109\n\t"
  "v_max3_f32 v116, v116, v110, v111\n\t"
  "v_max_f32 v116, v116, v117\n\t"
  "s_waitcnt lgkmcnt(0)\n\t"
  "s_mov_b32 s80, 0\n\t"
  "buffer_load_dword v13, v3, s[40:43], s54 offen sc1\n\t"
  "s_waitcnt vmcnt(1)\n\t"
  "v_mov_b32 v9, s83\n\t"
  "s_mov_b64 exec, 1\n\t"
  "buffer_store_dword v9, off, s[40:43], s47 sc1\n\t"
  "s_mov_b64 exec, -1\n\t"
  "s_waitcnt vmcnt(1)\n\t"
  "v_cmp_ge_i32 vcc, s83, v13\n\t"
  "s_cbranch_vccz .Latt_inok\n\t"
  ".Latt_in:\n\t"
  "buffer_load_dword v13, v3, s[40:43], s54 offen sc1\n\t"
  "s_waitcnt vmcnt(0)\n\t"
  "v_cmp_ge_i32 vcc, s83, v13\n\t"
  "s_cbranch_vccz .Latt_inok\n\t"
  "s_sleep 3\n\t"
  "s_add_u32 s80, s80, 1\n\t"
  "s_and_b32 s81, s80, 255\n\t"
  "s_cmp_lg_u32 s81, 0\n\t"
  "s_cbranch_scc1 .Latt_in\n\t"
  "buffer_load_dword v9, off, s[40:43], s49 sc1\n\t"
  "s_waitcnt vmcnt(0)\n\t"
  "v_readfirstlane_b32 s81, v9\n\t"
  "s_nop 1\n\t"
  "s_cmp_lg_u32 s81, 0\n\t"
  "s_cbranch_scc1 .Latt_end\n\t"
  "s_cmp_lt_u32 s80, 0x80000\n\t"
  "s_cbranch_scc1 .Latt_in\n\t"
  "v_mov_b32 v9, 1\n\t"
  "buffer_store_dword v9, off, s[40:43], s49 sc1\n\t"
  "s_branch .Latt_end\n\t"
  ".Latt_inok:\n\t"
  "s_lshl_b32 s81, s83, 18\n\t"
  "s_add_u32 s81, s81, s48\n\t"
  "s_lshr_b32 s82, s81, 21\n\t"
  "s_bitcmp1_b64 s[56:57], s82\n\t"
  "s_cselect_b32 s82, 0x2000, 0\n\t"
  "s_add_u32 s81, s81, s82\n\t"
  "s_mov_b32 m0, s50\n\t"
  "s_nop 0\n\t"
  "buffer_load_dwordx4 v0, s[40:43], s81 offen sc1 lds\n\t"
  "buffer_load_dwordx4 v0, s[40:43], s81 offen offset:1024 sc1 lds\n\t"
  "buffer_load_dwordx2 v[80:81], v7, s[40:43], s81 offen offset:2048 sc1\n\t"
  "buffer_load_dwordx2 v[82:83], v7, s[40:43], s81 offen offset:2112 sc1\n\t"
  "buffer_load_dwordx2 v[84:85], v7, s[40:43], s81 offen offset:2560 sc1\n\t"
  "buffer_load_dwordx2 v[86:87], v7, s[40:43], s81 offen offset:2624 sc1\n\t"
  "v_mov_b32 v9, s83\n\t"
  "s_mov_b64 exec, 1\n\t"
  "buffer_store_dword v9, off, s[40:43], s47 sc1\n\t"
  "s_mov_b64 exec, -1\n\t"
  "s_waitcnt vmcnt(5)\n\t"
  "s_branch .Latt_loop\n\t"
  ".Latt_fin:\n\t"
  "s_waitcnt vmcnt(0)\n\t"
  "v_mov_b32 v9, 64\n\t"
  "s_mov_b64 exec, 1\n\t"
  "buffer_store_dword v9, off, s[40:43], s47 sc1\n\t"
  "s_mov_b64 exec, -1\n\t"
  ".Latt_end:\n\t"
  "s_waitcnt vmcnt(0) lgkmcnt(0)\n\t"
    : "+{v[0:15]}"(cv)
    : "{a[0:31]}"(A[0]), "{a[32:63]}"(A[1]), "{a[64:95]}"(A[2]), "{a[96:127]}"(A[3]), "{a[128:159]}"(A[4]), "{a[160:191]}"(A[5]), "{a[192:223]}"(A[6]), "{a[224:255]}"(A[7]), "{s[40:43]}"(rw), "{s[44:59]}"(cs), "{s[60:75]}"(cm)
    : "memory", "vcc", "scc", "v16", "v17", "v18", "v19", "v20", "v21", "v22", "v23", "v24", "v25", "v26", "v27", "v28", "v29", "v30", "v31", "v32", "v33", "v34", "v35", "v36", "v37", "v38", "v39", "v40", "v41", "v42", "v43", "v44", "v45", "v46", "v47", "v48", "v49", "v50", "v51", "v52", "v53", "v54", "v55", "v56", "v57", "v58", "v59", "v60", "v61", "v62", "v63", "v64", "v65", "v66", "v67", "v68", "v69", "v70", "v71", "v72", "v73", "v74", "v75", "v76", "v77", "v78", "v79", "v80", "v81", "v82", "v83", "v84", "v85", "v86", "v87", "v88", "v89", "v90", "v91", "v92", "v93", "v94", "v95", "v96", "v97", "v98", "v99", "v100", "v101", "v102", "v103", "v104", "v105", "v106", "v107", "v108", "v109", "v110", "v111", "v112", "v113", "v114", "v115", "v116", "v117", "v118", "v119", "v120", "v121", "v122", "v123", "v124", "v125", "v126", "v127", "v128", "v129", "v130", "v131", "v132", "v133", "v134", "v135", "v136", "v137", "v138", "v139", "v140", "v141", "v142", "v143", "v144", "v145", "v146", "v147", "v148", "v149", "v150", "v151", "v152", "v153", "v154", "v155", "v156", "v157", "v158", "v159", "v160", "v161", "v162", "v163", "v164", "v165", "v166", "v167", "v168", "v169", "v170", "v171", "v172", "v173", "v174", "v175", "v176", "v177", "v178", "v179", "v180", "v181", "v182", "v183", "v184", "v185", "v186", "v187", "v188", "v189", "v190", "v191", "v192", "v193", "v194", "v195", "v196", "v197", "v198", "v199", "v200", "v201", "v202", "v203", "v204", "v205", "v206", "v207", "s80", "s81", "s82", "s83", "s84", "s85", "s86", "s87", "s88", "s89", "s90", "s91", "s92", "s93", "s94", "s95", "s96", "s97");
  __builtin_amdgcn_endpgm();
}

template <int NP> DEV void sum_partials(rsrc_t rw, unsigned vo, unsigned so, float (&acc)[8]) {
  u32x4 v[NP];
#pragma unroll
  for (int i = 0; i < NP; ++i) v[i] = ld128(rw, vo + hm(i * 4096), so);
#pragma unroll
  for (int j = 0; j < 8; ++j) acc[j] = 0.f;
#pragma unroll
  for (int i = 0; i < NP; ++i) {
#pragma unroll
    for (int j = 0; j < 4; ++j) { acc[2 * j] += bflo(v[i][j]); acc[2 * j + 1] += bfhi(v[i][j]); }
  }
}
DEV float row_sum(float v, LAS float* red, int w, int lane) {
  v += xor4f(v); v += xor8f(v); v = add32(add16(v));
  if (lane < 4) red[w * 4 + lane] = v;
  BARRIER();
  const int r = lane & 3;
  return (red[r] + red[4 + r]) + (red[8 + r] + red[12 + r]);
}
DEV void role_red1(const Params& p, char* wb, rsrc_t rw, int b, lds_t L, int tid) {
  const int lane = tid & 63, w = __builtin_amdgcn_readfirstlane(tid >> 6);
  const int team = b >> 3, rg = b & 7, dc = tid >> 2, row = rg * 4 + (tid & 3);
  const float* mod = (const float*)(wb + OFF_MOD) + dc * 8;
  float ga[8], cb[8], sb[8];
#pragma unroll
  for (int j = 0; j < 8; ++j) { ga[j] = mod[1024 + j]; sb[j] = mod[1536 + j]; cb[j] = 1.f + mod[2048 + j]; }
  const Near nr = get_near(rw, lane);
  bool dead = false;
  for (int s = team; s < NSEG; s += 2) {
    const u32x4 xv = *(const u32x4*)(p.x + (size_t)(s * 32 + row) * 512 + dc * 8);
    dead = block_wait(rw, fa(nr, F_ATT + (rg >> 2) + 2 * (lane >> 2)) + (lane & 3) * 64, (unsigned)(s + 1), w, dead);
    const unsigned sb_ = segbase(nr, s);
    STAMP(64 + b, 72 + s);
    float acc[8];
    sum_partials<16>(rw, hm((unsigned)rg * 65536) + tid * 16, sb_, acc);
    float sum = 0.f;
#pragma unroll
    for (int j = 0; j < 4; ++j) {
      acc[2 * j] = rbf(bflo(xv[j]) + ga[2 * j] * acc[2 * j]); acc[2 * j + 1] = rbf(bfhi(xv[j]) + ga[2 * j + 1] * acc[2 * j + 1]);
      sum += acc[2 * j] + acc[2 * j + 1];
    }
    const u32x4 x1 = {pk2(acc[0], acc[1]), pk2(acc[2], acc[3]), pk2(acc[4], acc[5]), pk2(acc[6], acc[7])};
    st128(x1, rw, hm(S_X1 + (unsigned)rg * 4096) + tid * 16, sb_);
    const float mean = row_sum(sum, (LAS float*)L, w, lane) * (1.f / 512.f);
    float q = 0.f;
#pragma unroll
    for (int j = 0; j < 8; ++j) { acc[j] -= mean; q += acc[j] * acc[j]; }
    const float rstd = 1.f / sqrtf(row_sum(q, (LAS float*)(L + 64), w, lane) * (1.f / 512.f) + 1e-5f);
#pragma unroll
    for (int j = 0; j < 8; ++j) acc[j] = acc[j] * rstd * cb[j] + sb[j];
    const u32x4 h2 = {pk2(acc[0], acc[1]), pk2(acc[2], acc[3]), pk2(acc[4], acc[5]), pk2(acc[6], acc[7])};
    st128(h2, rw, hm(S_H2 + (unsigned)(((row >> 4) * 16 + (dc >> 2)) * 1024 + ((dc & 3) * 16 + (row & 15)) * 16)), sb_);
    block_publish(rw, fa(nr, F_R1 + b), (unsigned)(s + 1), tid);
    STAMP(64 + b, 8 + s);
  }
}
DEV void role_red2(const Params& p, char* wb, rsrc_t rw, int b, int tid) {
  const int lane = tid & 63, w = __builtin_amdgcn_readfirstlane(tid >> 6);
  const int team = b >> 3, rg = b & 7, dc = tid >> 2, row = rg * 4 + (tid & 3);
  const float* mod = (const float*)(wb + OFF_MOD) + dc * 8;
  float gb[8], b2[8];
#pragma unroll
  for (int j = 0; j < 8; ++j) { gb[j] = mod[2560 + j]; b2[j] = bf2f(p.b2[dc * 8 + j]); }
  const Near nr = get_near(rw, lane);
  bool dead = false;
  for (int s = team; s < NSEG; s += 2) {
    dead = block_wait(rw, fa(nr, F_FFN + (rg >> 2) + 2 * min(lane >> 2, 13)) + (lane & 3) * 64, (unsigned)(s + 1), w, dead);
    const unsigned sb_ = segbase(nr, s);
    STAMP(80 + b, 72 + s);
    const u32x4 xv = ld128(rw, hm(S_X1 + (unsigned)rg * 4096) + tid * 16, sb_);
    float acc[8];
    sum_partials<14>(rw, hm(S_PF + (unsigned)rg * (14 * 4096)) + tid * 16, sb_, acc);
#pragma unroll
    for (int j = 0; j < 4; ++j) {
      acc[2 * j] = bflo(xv[j]) + gb[2 * j] * (acc[2 * j] + b2[2 * j]); acc[2 * j + 1] = bfhi(xv[j]) + gb[2 * j + 1] * (acc[2 * j + 1] + b2[2 * j + 1]);
    }
    const u32x4 o = {pk2(acc[0], acc[1]), pk2(acc[2], acc[3]), pk2(acc[4], acc[5]), pk2(acc[6], acc[7])};
    *(u32x4*)(p.out + (size_t)(s * 32 + row) * 512 + dc * 8) = o;
    STAMP(80 + b, 8 + s);
  }
}

DEV void role_ffn(const Params& p, char* wb, rsrc_t rw, int b, lds_t L, int tid) {
  const int lane = tid & 63, w = __builtin_amdgcn_readfirstlane(tid >> 6);
  const unsigned l0 = (unsigned)(uintptr_t)L;
  const int gb = lane >> 4, qq = (lane >> 2) & 3, pp = lane & 3, n16 = lane & 15;
  const int f = b >> 1, tt = b & 1;
  u32x32 A[8];
  const char* w1 = (const char*)(p.w1 + f * 128);
  const char* w2 = (const char*)(p.w2 + (size_t)f * 128 * 512);
  stage_chunk<256, 0>(w1, 3584, l0, w, lane);
  stage_chunk<256, 0>(w1 + (size_t)256 * 3584, 3584, l0 + 65536, w, lane);
  const unsigned rid = (unsigned)(qq | ((gb & 1) << 2));
#pragma unroll
  for (int ck = 0; ck < 4; ++ck) {
    if (ck < 3) VMWAIT(16); else VMWAIT(0);
    BARRIER();
    if (ck < 2) {
      const unsigned chunk = (unsigned)(w * 4 + pp) ^ (rid << 1);
#pragma unroll
      for (int t = 0; t < 2; ++t) {
        const lds_t bb = L + ck * 65536 + (8 * gb + qq) * 256 + chunk * 16 + t * 8;
#pragma unroll
        for (int u = 0; u < 16; ++u) {
          const u32x2 v = ldstr(bb + (u >> 1) * 8192 + (u & 1) * 1024);
          A[t * 2 + ck][u * 2] = v.x; A[t * 2 + ck][u * 2 + 1] = v.y;
        }
      }
    } else {
      read_cols128<4, 128>(A, 2 * (ck - 2), L + (ck & 1) * 65536, w, lane);
    }
    BARRIER();
    if (ck < 2) stage_chunk<1024, 0>(w2 + ck * 65536, 1024, l0 + ck * 65536, w, lane);
  }
  const Near nr = get_near(rw, lane);
  u32x8 b1, cs;
#pragma unroll
  for (int j = 0; j < 8; ++j) b1[j] = (unsigned)p.b1[f * 128 + w * 32 + gb * 8 + j] << 16;
  const u32x4 cv = {(unsigned)lane * 16, l0 + (unsigned)lane * 16, hm(S_PF + (unsigned)(((tt * 4 + (n16 >> 2)) * 14 + f) * 4096 + (16 * w + gb) * 64 + (n16 & 3) * 16)), FS * (unsigned)(lane & 3)};
  cs[0] = OFF_SEG + hm(S_H2 + (unsigned)(tt * 16384 + w * 4096));
  cs[1] = l0 + (unsigned)w * 4096;
  cs[2] = OFF_SEG;
  cs[3] = fa(nr, F_FFN + b) + 64 * (unsigned)w;
  cs[4] = fa(nr, F_R1 + 4 * tt);
  cs[5] = OFF_CTRL + C_ERR;
  cs[6] = 32768 + (unsigned)w * 1024;
  cs[7] = w == 0 ? OFF_DBG + (unsigned)((176 + b) * 256 + 8) * 8 : 0xf0000000u;
  asm volatile(
  "s_mov_b32 s83, 0\n\t"
  "s_mov_b32 s84, 0\n\t"
  "s_mov_b32 s85, 0\n\t"
  "s_mov_b32 s60, 0xbdd2d3e8\n\t"
  "s_mov_b32 s61, s60\n\t"
  "v_mov_b32 v6, 0xc0135761\n\t"
  "v_mov_b32 v7, v6\n\t"
  "v_add_u32 v8, s50, v1\n\t"
  "s_mov_b32 s80, 0\n\t"
  ".Lffn_i0:\n\t"
  "buffer_load_dword v5, v3, s[40:43], s48 offen sc1\n\t"
  "s_waitcnt vmcnt(0)\n\t"
  "v_cmp_ge_i32 vcc, s83, v5\n\t"
  "s_cbranch_vccz .Lffn_i0ok\n\t"
  "s_sleep 3\n\t"
  "s_add_u32 s80, s80, 1\n\t"
  "s_and_b32 s81, s80, 255\n\t"
  "s_cmp_lg_u32 s81, 0\n\t"
  "s_cbranch_scc1 .Lffn_i0\n\t"
  "buffer_load_dword v9, off, s[40:43], s49 sc1\n\t"
  "s_waitcnt vmcnt(0)\n\t"
  "v_readfirstlane_b32 s81, v9\n\t"
  "s_nop 1\n\t"
  "s_cmp_lg_u32 s81, 0\n\t"
  "s_cbranch_scc1 .Lffn_end\n\t"
  "s_cmp_lt_u32 s80, 0x80000\n\t"
  "s_cbranch_scc1 .Lffn_i0\n\t"
  "v_mov_b32 v9, 1\n\t"
  "buffer_store_dword v9, off, s[40:43], s49 sc1\n\t"
  "s_branch .Lffn_end\n\t"
  ".Lffn_i0ok:\n\t"
  "s_lshl_b32 s81, s83, 21\n\t"
  "s_add_u32 s81, s81, s44\n\t"
  "s_bitcmp1_b64 s[58:59], s83\n\t"
  "s_cselect_b32 s82, 0x2000, 0\n\t"
  "s_add_u32 s81, s81, s82\n\t"
  "s_and_b32 s82, s83, 1\n\t"
  "s_lshl_b32 s82, s82, 14\n\t"
  "s_add_u32 m0, s82, s45\n\t"
  "s_nop 0\n\t"
  "buffer_load_dwordx4 v0, s[40:43], s81 offen offset:0 sc1 lds\n\t"
  "buffer_load_dwordx4 v0, s[40:43], s81 offen offset:1024 sc1 lds\n\t"
  "buffer_load_dwordx4 v0, s[40:43], s81 offen offset:2048 sc1 lds\n\t"
  "buffer_load_dwordx4 v0, s[40:43], s81 offen offset:3072 sc1 lds\n\t"
  "s_waitcnt vmcnt(0)\n\t"
  ".Lffn_loop:\n\t"
  "s_and_b32 s62, s83, 1\n\t"
  "s_lshl_b32 s63, s62, 14\n\t"
  "v_add_u32 v4, s63, v1\n\t"
  "s_lshl_b32 s62, s62, 11\n\t"
  "s_xor_b32 s62, s62, 0x800\n\t"
  "s_add_u32 s62, s62, s48\n\t"
  ".Lffn_go:\n\t"
  "s_barrier\n\t"
  "ds_read_b128 v[32:35], v4 offset:0\n\t"
  "ds_read_b128 v[36:39], v4 offset:1024\n\t"
  "ds_read_b128 v[40:43], v4 offset:2048\n\t"
  "ds_read_b128 v[44:47], v4 offset:3072\n\t"
  "ds_read_b128 v[48:51], v4 offset:4096\n\t"
  "s_waitcnt lgkmcnt(4)\n\t"
  "v_mfma_f32_16x16x32_bf16 v[20:23], a[0:3], v[32:35], v[12:15]\n\t"
  "ds_read_b128 v[52:55], v4 offset:5120\n\t"
  "v_mfma_f32_16x16x32_bf16 v[24:27], a[64:67], v[32:35], v[16:19]\n\t"
  "s_waitcnt lgkmcnt(4)\n\t"
  "v_mfma_f32_16x16x32_bf16 v[20:23], a[4:7], v[36:39], v[20:23]\n\t"
  "ds_read_b128 v[32:35], v4 offset:6144\n\t"
  "v_mfma_f32_16x16x32_bf16 v[24:27], a[68:71], v[36:39], v[24:27]\n\t"
  "s_waitcnt lgkmcnt(4)\n\t"
  "v_mfma_f32_16x16x32_bf16 v[20:23], a[8:11], v[40:43], v[20:23]\n\t"
  "ds_read_b128 v[36:39], v4 offset:7168\n\t"
  "v_mfma_f32_16x16x32_bf16 v[24:27], a[72:75], v[40:43], v[24:27]\n\t"
  "s_waitcnt lgkmcnt(4)\n\t"
  "v_mfma_f32_16x16x32_bf16 v[20:23], a[12:15], v[44:47], v[20:23]\n\t"
  "ds_read_b128 v[40:43], v4 offset:8192\n\t"
  "v_mfma_f32_16x16x32_bf16 v[24:27], a[76:79], v[44:47], v[24:27]\n\t"
  "s_waitcnt lgkmcnt(4)\n\t"
  "v_mfma_f32_16x16x32_bf16 v[20:23], a[16:19], v[48:51], v[20:23]\n\t"
  "ds_read_b128 v[44:47], v4 offset:9216\n\t"
  "v_mfma_f32_16x16x32_bf16 v[24:27], a[80:83], v[48:51], v[24:27]\n\t"
  "s_waitcnt lgkmcnt(4)\n\t"
  "v_mfma_f32_16x16x32_bf16 v[20:23], a[20:23], v[52:55], v[20:23]\n\t"
  "ds_read_b128 v[48:51], v4 offset:10240\n\t"
  "v_mfma_f32_16x16x32_bf16 v[24:27], a[84:87], v[52:55], v[24:27]\n\t"
  "s_waitcnt lgkmcnt(4)\n\t"
  "v_mfma_f32_16x16x32_bf16 v[20:23], a[24:27], v[32:35], v[20:23]\n\t"
  "ds_read_b128 v[52:55], v4 offset:11264\n\t"
  "v_mfma_f32_16x16x32_bf16 v[24:27], a[88:91], v[32:35], v[24:27]\n\t"
  "s_waitcnt lgkmcnt(4)\n\t"
  "v_mfma_f32_16x16x32_bf16 v[20:23], a[28:31], v[36:39], v[20:23]\n\t"
  "ds_read_b128 v[32:35], v4 offset:12288\n\t"
  "v_mfma_f32_16x16x32_bf16 v[24:27], a[92:95], v[36:39], v[24:27]\n\t"
  "s_waitcnt lgkmcnt(4)\n\t"
  "v_mfma_f32_16x16x32_bf16 v[20:23], a[32:35], v[40:43], v[20:23]\n\t"
  "ds_read_b128 v[36:39], v4 offset:13312\n\t"
  "v_mfma_f32_16x16x32_bf16 v[24:27], a[96:99], v[40:43], v[24:27]\n\t"
  "s_waitcnt lgkmcnt(4)\n\t"
  "v_mfma_f32_16x16x32_bf16 v[20:23], a[36:39], v[44:47], v[20:23]\n\t"
  "ds_read_b128 v[40:43], v4 offset:14336\n\t"
  "v_mfma_f32_16x16x32_bf16 v[24:27], a[100:103], v[44:47], v[24:27]\n\t"
  "s_waitcnt lgkmcnt(4)\n\t"
  "v_mfma_f32_16x16x32_bf16 v[20:23], a[40:43], v[48:51], v[20:23]\n\t"
  "ds_read_b128 v[44:47], v4 offset:15360\n\t"
  "v_mfma_f32_16x16x32_bf16 v[24:27], a[104:107], v[48:51], v[24:27]\n\t"
  "s_waitcnt lgkmcnt(4)\n\t"
  "v_mfma_f32_16x16x32_bf16 v[20:23], a[44:47], v[52:55], v[20:23]\n\t"
  "v_mfma_f32_16x16x32_bf16 v[24:27], a[108:111], v[52:55], v[24:27]\n\t"
  "s_waitcnt lgkmcnt(3)\n\t"
  "v_mfma_f32_16x16x32_bf16 v[20:23], a[48:51], v[32:35], v[20:23]\n\t"
  "v_mfma_f32_16x16x32_bf16 v[24:27], a[112:115], v[32:35], v[24:27]\n\t"
  "s_waitcnt lgkmcnt(2)\n\t"
  "v_mfma_f32_16x16x32_bf16 v[20:23], a[52:55], v[36:39], v[20:23]\n\t"
  "v_mfma_f32_16x16x32_bf16 v[24:27], a[116:119], v[36:39], v[24:27]\n\t"
  "s_waitcnt lgkmcnt(1)\n\t"
  "v_mfma_f32_16x16x32_bf16 v[20:23], a[56:59], v[40:43], v[20:23]\n\t"
  "v_mfma_f32_16x16x32_bf16 v[24:27], a[120:123], v[40:43], v[24:27]\n\t"
  "s_waitcnt lgkmcnt(0)\n\t"
  "v_mfma_f32_16x16x32_bf16 v[20:23], a[60:63], v[44:47], v[20:23]\n\t"
  "v_mfma_f32_16x16x32_bf16 v[24:27], a[124:127], v[44:47], v[24:27]\n\t"
  "s_nop 4\n\t"
  "v_pk_mul_f32 v[32:33], v[20:21], v[20:21]\n\t"
  "v_pk_mul_f32 v[34:35], v[22:23], v[22:23]\n\t"
  "s_nop 0\n\t"
  "v_pk_mul_f32 v[36:37], v[24:25], v[24:25]\n\t"
  "v_pk_mul_f32 v[38:39], v[26:27], v[26:27]\n\t"
  "v_pk_fma_f32 v[32:33], v[32:33], s[60:61], v[6:7]\n\t"
  "v_pk_fma_f32 v[34:35], v[34:35], s[60:61], v[6:7]\n\t"
  "v_pk_fma_f32 v[36:37], v[36:37], s[60:61], v[6:7]\n\t"
  "v_pk_fma_f32 v[38:39], v[38:39], s[60:61], v[6:7]\n\t"
  "v_pk_mul_f32 v[32:33], v[32:33], v[20:21]\n\t"
  "v_pk_mul_f32 v[34:35], v[34:35], v[22:23]\n\t"
  "v_pk_mul_f32 v[36:37], v[36:37], v[24:25]\n\t"
  "v_pk_mul_f32 v[38:39], v[38:39], v[26:27]\n\t"
  "v_exp_f32 v32, v32\n\t"
  "v_exp_f32 v33, v33\n\t"
  "v_exp_f32 v34, v34\n\t"
  "v_exp_f32 v35, v35\n\t"
  "v_exp_f32 v36, v36\n\t"
  "v_exp_f32 v37, v37\n\t"
  "v_exp_f32 v38, v38\n\t"
  "v_exp_f32 v39, v39\n\t"
  "v_pk_add_f32 v[32:33], v[32:33], 1.0 op_sel_hi:[1,0]\n\t"
  "v_pk_add_f32 v[34:35], v[34:35], 1.0 op_sel_hi:[1,0]\n\t"
  "v_pk_add_f32 v[36:37], v[36:37], 1.0 op_sel_hi:[1,0]\n\t"
  "v_pk_add_f32 v[38:39], v[38:39], 1.0 op_sel_hi:[1,0]\n\t"
  "v_rcp_f32 v32, v32\n\t"
  "v_rcp_f32 v33, v33\n\t"
  "v_rcp_f32 v34, v34\n\t"
  "v_rcp_f32 v35, v35\n\t"
  "v_rcp_f32 v36, v36\n\t"
  "v_rcp_f32 v37, v37\n\t"
  "v_rcp_f32 v38, v38\n\t"
  "v_rcp_f32 v39, v39\n\t"
  "v_pk_mul_f32 v[32:33], v[32:33], v[20:21]\n\t"
  "v_pk_mul_f32 v[34:35], v[34:35], v[22:23]\n\t"
  "v_pk_mul_f32 v[36:37], v[36:37], v[24:25]\n\t"
  "v_pk_mul_f32 v[38:39], v[38:39], v[26:27]\n\t"
  "v_cvt_pk_bf16_f32 v40, v32, v33\n\t"
  "v_cvt_pk_bf16_f32 v41, v34, v35\n\t"
  "v_cvt_pk_bf16_f32 v42, v36, v37\n\t"
  "v_cvt_pk_bf16_f32 v43, v38, v39\n\t"
  "ds_write_b128 v8, v[40:43]\n\t"
  "buffer_load_dword v5, v3, s[40:43], s62 offen sc1\n\t"
  "s_add_u32 s64, s83, 1\n\t"
  "s_lshl_b32 s65, s83, 21\n\t"
  "s_add_u32 s65, s65, s46\n\t"
  "s_bitcmp1_b64 s[58:59], s83\n\t"
  "s_cselect_b32 s81, 0x2000, 0\n\t"
  "s_add_u32 s65, s65, s81\n\t"
  "s_waitcnt lgkmcnt(0)\n\t"
  "s_barrier\n\t"
  "ds_read_b128 v[48:51], v1 offset:32768\n\t"
  "ds_read_b128 v[52:55], v1 offset:33792\n\t"
  "ds_read_b128 v[56:59], v1 offset:34816\n\t"
  "ds_read_b128 v[60:63], v1 offset:35840\n\t"
  "s_waitcnt lgkmcnt(3)\n\t"
  "v_mfma_f32_16x16x32_bf16 v[64:67], a[128:131], v[48:51], 0\n\t"
  "v_mfma_f32_16x16x32_bf16 v[68:71], a[144:147], v[48:51], 0\n\t"
  "s_waitcnt lgkmcnt(2)\n\t"
  "v_mfma_f32_16x16x32_bf16 v[64:67], a[132:135], v[52:55], v[64:67]\n\t"
  "v_mfma_f32_16x16x32_bf16 v[68:71], a[148:151], v[52:55], v[68:71]\n\t"
  "s_waitcnt lgkmcnt(1)\n\t"
  "v_mfma_f32_16x16x32_bf16 v[64:67], a[136:139], v[56:59], v[64:67]\n\t"
  "v_mfma_f32_16x16x32_bf16 v[68:71], a[152:155], v[56:59], v[68:71]\n\t"
  "s_waitcnt lgkmcnt(0)\n\t"
  "v_mfma_f32_16x16x32_bf16 v[64:67], a[140:143], v[60:63], v[64:67]\n\t"
  "v_mfma_f32_16x16x32_bf16 v[68:71], a[156:159], v[60:63], v[68:71]\n\t"
  "v_mfma_f32_16x16x32_bf16 v[72:75], a[160:163], v[48:51], 0\n\t"
  "v_mfma_f32_16x16x32_bf16 v[76:79], a[176:179], v[48:51], 0\n\t"
  "v_mfma_f32_16x16x32_bf16 v[72:75], a[164:167], v[52:55], v[72:75]\n\t"
  "v_cvt_pk_bf16_f32 v80, v64, v65\n\t"
  "v_cvt_pk_bf16_f32 v81, v66, v67\n\t"
  "v_mfma_f32_16x16x32_bf16 v[76:79], a[180:183], v[52:55], v[76:79]\n\t"
  "v_cvt_pk_bf16_f32 v82, v68, v69\n\t"
  "v_cvt_pk_bf16_f32 v83, v70, v71\n\t"
  "v_mfma_f32_16x16x32_bf16 v[72:75], a[168:171], v[56:59], v[72:75]\n\t"
  "buffer_store_dwordx4 v[80:83], v2, s[40:43], s65 offen offset:0 sc1\n\t"
  "v_mfma_f32_16x16x32_bf16 v[76:79], a[184:187], v[56:59], v[76:79]\n\t"
  "v_mfma_f32_16x16x32_bf16 v[72:75], a[172:175], v[60:63], v[72:75]\n\t"
  "v_mfma_f32_16x16x32_bf16 v[76:79], a[188:191], v[60:63], v[76:79]\n\t"
  "v_mfma_f32_16x16x32_bf16 v[64:67], a[192:195], v[48:51], 0\n\t"
  "v_mfma_f32_16x16x32_bf16 v[68:71], a[208:211], v[48:51], 0\n\t"
  "v_mfma_f32_16x16x32_bf16 v[64:67], a[196:199], v[52:55], v[64:67]\n\t"
  "v_cvt_pk_bf16_f32 v84, v72, v73\n\t"
  "v_cvt_pk_bf16_f32 v85, v74, v75\n\t"
  "v_mfma_f32_16x16x32_bf16 v[68:71], a[212:215], v[52:55], v[68:71]\n\t"
  "v_cvt_pk_bf16_f32 v86, v76, v77\n\t"
  "v_cvt_pk_bf16_f32 v87, v78, v79\n\t"
  "v_mfma_f32_16x16x32_bf16 v[64:67], a[200:203], v[56:59], v[64:67]\n\t"
  "buffer_store_dwordx4 v[84:87], v2, s[40:43], s65 offen offset:256 sc1\n\t"
  "v_mfma_f32_16x16x32_bf16 v[68:71], a[216:219], v[56:59], v[68:71]\n\t"
  "v_mfma_f32_16x16x32_bf16 v[64:67], a[204:207], v[60:63], v[64:67]\n\t"
  "v_mfma_f32_16x16x32_bf16 v[68:71], a[220:223], v[60:63], v[68:71]\n\t"
  "v_mfma_f32_16x16x32_bf16 v[72:75], a[224:227], v[48:51], 0\n\t"
  "v_mfma_f32_16x16x32_bf16 v[76:79], a[240:243], v[48:51], 0\n\t"
  "v_mfma_f32_16x16x32_bf16 v[72:75], a[228:231], v[52:55], v[72:75]\n\t"
  "v_cvt_pk_bf16_f32 v80, v64, v65\n\t"
  "v_cvt_pk_bf16_f32 v81, v66, v67\n\t"
  "v_mfma_f32_16x16x32_bf16 v[76:79], a[244:247], v[52:55], v[76:79]\n\t"
  "v_cvt_pk_bf16_f32 v82, v68, v69\n\t"
  "v_cvt_pk_bf16_f32 v83, v70, v71\n\t"
  "v_mfma_f32_16x16x32_bf16 v[72:75], a[232:235], v[56:59], v[72:75]\n\t"
  "buffer_store_dwordx4 v[80:83], v2, s[40:43], s65 offen offset:512 sc1\n\t"
  "v_mfma_f32_16x16x32_bf16 v[76:79], a[248:251], v[56:59], v[76:79]\n\t"
  "v_mfma_f32_16x16x32_bf16 v[72:75], a[236:239], v[60:63], v[72:75]\n\t"
  "v_mfma_f32_16x16x32_bf16 v[76:79], a[252:255], v[60:63], v[76:79]\n\t"
  "s_nop 4\n\t"
  "v_cvt_pk_bf16_f32 v84, v72, v73\n\t"
  "v_cvt_pk_bf16_f32 v85, v74, v75\n\t"
  "s_nop 0\n\t"
  "v_cvt_pk_bf16_f32 v86, v76, v77\n\t"
  "v_cvt_pk_bf16_f32 v87, v78, v79\n\t"
  "buffer_store_dwordx4 v[84:87], v2, s[40:43], s65 offen offset:768 sc1\n\t"
  ".Lffn_nx:\n\t"
  "s_cmp_eq_u32 s83, 63\n\t"
  "s_cbranch_scc1 .Lffn_fin\n\t"
  "s_waitcnt vmcnt(4)\n\t"
  "v_cmp_ge_i32 vcc, s64, v5\n\t"
  "s_mov_b32 s83, s64\n\t"
  "s_cbranch_vccnz .Lffn_wait\n\t"
  "s_lshl_b32 s81, s83, 21\n\t"
  "s_add_u32 s81, s81, s44\n\t"
  "s_bitcmp1_b64 s[58:59], s83\n\t"
  "s_cselect_b32 s82, 0x2000, 0\n\t"
  "s_add_u32 s81, s81, s82\n\t"
  "s_and_b32 s82, s83, 1\n\t"
  "s_lshl_b32 s82, s82, 14\n\t"
  "s_add_u32 m0, s82, s45\n\t"
  "s_nop 0\n\t"
  "buffer_load_dwordx4 v0, s[40:43], s81 offen offset:0 sc1 lds\n\t"
  "buffer_load_dwordx4 v0, s[40:43], s81 offen offset:1024 sc1 lds\n\t"
  "buffer_load_dwordx4 v0, s[40:43], s81 offen offset:2048 sc1 lds\n\t"
  "buffer_load_dwordx4 v0, s[40:43], s81 offen offset:3072 sc1 lds\n\t"
  "s_waitcnt vmcnt(4)\n\t"
  "v_mov_b32 v9, s83\n\t"
  "s_mov_b64 exec, 1\n\t"
  "buffer_store_dword v9, off, s[40:43], s47 sc1\n\t"
  "s_mov_b64 exec, -1\n\t"
  "s_waitcnt vmcnt(1)\n\t"
  "s_branch .Lffn_loop\n\t"
  ".Lffn_wait:\n\t"
  "s_mov_b32 s80, 0\n\t"
  "buffer_load_dword v5, v3, s[40:43], s62 offen sc1\n\t"
  "s_waitcnt vmcnt(1)\n\t"
  "v_mov_b32 v9, s83\n\t"
  "s_mov_b64 exec, 1\n\t"
  "buffer_store_dword v9, off, s[40:43], s47 sc1\n\t"
  "s_mov_b64 exec, -1\n\t"
  "s_waitcnt vmcnt(1)\n\t"
  "v_cmp_ge_i32 vcc, s83, v5\n\t"
  "s_cbranch_vccz .Lffn_inok\n\t"
  ".Lffn_in:\n\t"
  "buffer_load_dword v5, v3, s[40:43], s62 offen sc1\n\t"
  "s_waitcnt vmcnt(0)\n\t"
  "v_cmp_ge_i32 vcc, s83, v5\n\t"
  "s_cbranch_vccz .Lffn_inok\n\t"
  "s_sleep 3\n\t"
  "s_add_u32 s80, s80, 1\n\t"
  "s_and_b32 s81, s80, 255\n\t"
  "s_cmp_lg_u32 s81, 0\n\t"
  "s_cbranch_scc1 .Lffn_in\n\t"
  "buffer_load_dword v9, off, s[40:43], s49 sc1\n\t"
  "s_waitcnt vmcnt(0)\n\t"
  "v_readfirstlane_b32 s81, v9\n\t"
  "s_nop 1\n\t"
  "s_cmp_lg_u32 s81, 0\n\t"
  "s_cbranch_scc1 .Lffn_end\n\t"
  "s_cmp_lt_u32 s80, 0x80000\n\t"
  "s_cbranch_scc1 .Lffn_in\n\t"
  "v_mov_b32 v9, 1\n\t"
  "buffer_store_dword v9, off, s[40:43], s49 sc1\n\t"
  "s_branch .Lffn_end\n\t"
  ".Lffn_inok:\n\t"
  "s_lshl_b32 s81, s83, 21\n\t"
  "s_add_u32 s81, s81, s44\n\t"
  "s_bitcmp1_b64 s[58:59], s83\n\t"
  "s_cselect_b32 s82, 0x2000, 0\n\t"
  "s_add_u32 s81, s81, s82\n\t"
  "s_and_b32 s82, s83, 1\n\t"
  "s_lshl_b32 s82, s82, 14\n\t"
  "s_add_u32 m0, s82, s45\n\t"
  "s_nop 0\n\t"
  "buffer_load_dwordx4 v0, s[40:43], s81 offen offset:0 sc1 lds\n\t"
  "buffer_load_dwordx4 v0, s[40:43], s81 offen offset:1024 sc1 lds\n\t"
  "buffer_load_dwordx4 v0, s[40:43], s81 offen offset:2048 sc1 lds\n\t"
  "buffer_load_dwordx4 v0, s[40:43], s81 offen offset:3072 sc1 lds\n\t"
  "s_waitcnt vmcnt(0)\n\t"
  "s_branch .Lffn_loop\n\t"
  ".Lffn_fin:\n\t"
  "s_waitcnt vmcnt(0)\n\t"
  "v_mov_b32 v9, 64\n\t"
  "s_mov_b64 exec, 1\n\t"
  "buffer_store_dword v9, off, s[40:43], s47 sc1\n\t"
  "s_mov_b64 exec, -1\n\t"
  ".Lffn_end:\n\t"
  "s_waitcnt vmcnt(0) lgkmcnt(0)\n\t"
    :
    : "{a[0:31]}"(A[0]), "{a[32:63]}"(A[1]), "{a[64:95]}"(A[2]), "{a[96:127]}"(A[3]), "{a[128:159]}"(A[4]), "{a[160:191]}"(A[5]), "{a[192:223]}"(A[6]), "{a[224:255]}"(A[7]), "{s[40:43]}"(rw), "{s[44:51]}"(cs), "{s[58:59]}"(nr.ps), "{v[0:3]}"(cv), "{v[12:19]}"(b1)
    : "memory", "vcc", "scc", "v4", "v5", "v6", "v7", "v8", "v9", "v10", "v11", "v20", "v21", "v22", "v23", "v24", "v25", "v26", "v27", "v28", "v29", "v30", "v31", "v32", "v33", "v34", "v35", "v36", "v37", "v38", "v39", "v40", "v41", "v42", "v43", "v44", "v45", "v46", "v47", "v48", "v49", "v50", "v51", "v52", "v53", "v54", "v55", "v56", "v57", "v58", "v59", "v60", "v61", "v62", "v63", "v64", "v65", "v66", "v67", "v68", "v69", "v70", "v71", "v72", "v73", "v74", "v75", "v76", "v77", "v78", "v79", "v80", "v81", "v82", "v83", "v84", "v85", "v86", "v87", "v88", "v89", "v90", "v91", "v92", "v93", "v94", "v95", "s60", "s61", "s62", "s63", "s64", "s65", "s66", "s67", "s80", "s81", "s82", "s83", "s84", "s85", "s86", "s87", "s88", "s89");
  __builtin_amdgcn_endpgm();
}

DEV void role_warm(char* wb, rsrc_t rw, int hp, lds_t L, int tid) {
  const int lane = tid & 63, w = __builtin_amdgcn_readfirstlane(tid >> 6);
  const Near nr = get_near(rw, lane);
  const unsigned fo = lane == 2 ? fa(nr, F_REC + 4 * hp) : fa0(F_KV + (lane & 1) * 16 + hp);
  const unsigned vo = (unsigned)hp * 32768 + (unsigned)(w & 1) * 16384 + lane * 16;
  unsigned acc = 0;
  int seen = 0;
  bool dead = false;
  for (int s = 2; s < NSEG; ++s) {
    if (w == 0 && !dead) {
      const int want = lane == 2 ? s - WARM_LEAD : s + 1;
      for (unsigned sp = 0; !__all(seen >= want); ++sp) {
        CFENCE();
        if (sp) __builtin_amdgcn_s_sleep(16);
        seen = (int)ld32(rw, fo, 0);
        if ((sp & 63u) == 63u && (__builtin_amdgcn_readfirstlane(ld32(rw, OFF_CTRL + C_ERR, 0)) != 0u || sp > SPIN_CAP)) { dead = true; break; }
      }
    }
    BARRIER();
    const unsigned so = OFF_QKV + (unsigned)(1 + (w >> 1)) * 32 * MB + (unsigned)s * 524288;
#pragma unroll
    for (int i = 0; i < 8; ++i) { const u32x4 v = ld128(rw, vo + i * 1024, so); acc ^= v.x; }
    asm volatile("" :: "v"(acc));
  }
}

DEV int role_of(unsigned x, unsigned t) {
  if (t >= 32u) return -1;
  if (x < 4u) {
    if (t < 16u) return (int)(x * 16u + t);
    if (t < 20u) return (int)(80u + (t - 16u) * 4u + x);
    return t < 24u ? (int)(204u + x * 4u + (t - 20u)) : -1;
  }
  const unsigned b = t * 4u + (x - 4u);
  if (t < 12u) return (int)(96u + ((t >> 2) + 1u) % 3u * 16u + (x - 4u) * 4u + (t & 3u));
  return b < 108u ? (int)(96u + b) : (b < 124u ? (int)(b - 44u) : -1);
}

__global__ void __launch_bounds__(256, 1) k_main(Params p) {
  extern __shared__ __attribute__((aligned(16))) char smem[];
  const lds_t L = (lds_t)smem;
  char* wb = ws_base(p.ws);
  const rsrc_t rw = __builtin_amdgcn_make_buffer_rsrc(wb, 0, WS_END, 0x00020000);
  const int tid = threadIdx.x;
  if (blockIdx.x >= 128u && (blockIdx.x & 4u)) {
    role_ln1(p, rw, (int)(((blockIdx.x - 128u) >> 3) * 4u + (blockIdx.x & 3u)), L, tid);
    STAMP(254, (blockIdx.x - 128u) >> 1);
  }
  GAS unsigned* ctrl = (GAS unsigned*)(wb + OFF_CTRL);
  const unsigned x = xcc_id();
  const bool filler = blockIdx.x >= 128u && !(blockIdx.x & 4u);
  const int fj = (int)(((blockIdx.x - 128u) >> 3) * 4u + (blockIdx.x & 3u));
  if (filler) { role_fill(p, rw, fj, tid); if (!VOTE_LATE) { role_vote(rw, fj, tid); STAMP(252, fj); } else __builtin_amdgcn_s_sleep(16); }
  unsigned tk = 0;
  if (tid == 0) tk = __hip_atomic_fetch_add(ctrl + (C_TICKET + x * 256) / 4, 1u, __ATOMIC_RELAXED, __HIP_MEMORY_SCOPE_AGENT);
  if (tid == 0) *(LAS unsigned*)(L + 16) = tk;
  __syncthreads();
  const unsigned t = __builtin_amdgcn_readfirstlane(*(LAS unsigned*)(L + 16));
  if (x < 4u && t >= 16u && t < 32u) {
    touch<16>(p.wv + (size_t)(32 * (t - 16u) + (tid >> 7)) * 4096 + x * 1024 + (tid & 127) * 8, 8192);
    touch<16>(p.wo + (size_t)x * 524288 + (t - 16u) * 32768 + tid * 8, 2048);
  } else if (x >= 4u && t >= 8u && t < 16u) {
    touch<64 - 4 * PF_B>(((tid >> 7) ? p.wk : p.wv) + (size_t)(32 * PF_B + (64 - 4 * PF_B) * (t - 8u)) * 4096 + (x - 4u) * 1024 + (tid & 127) * 8, 4096);
  }
  if (filler && VOTE_LATE) { role_vote(rw, fj, tid); STAMP(252, fj); }
  int role = role_of(x, t);
  if (role < 0) {
  if (tid == 0) {
    {
      unsigned cnt[8];
      bool ok = false;
      for (unsigned sp = 0; sp < SPIN_CAP; ++sp) {
        unsigned tot = 0;
#pragma unroll
        for (int i = 0; i < 8; ++i) { cnt[i] = __hip_atomic_load(ctrl + (C_TICKET + i * 256) / 4, __ATOMIC_RELAXED, __HIP_MEMORY_SCOPE_AGENT); tot += cnt[i]; }
        if (tot >= gridDim.x) { ok = true; break; }
        __builtin_amdgcn_s_sleep(4);
      }
      if (ok) {
        bool any = false;
#pragma unroll
        for (int i = 0; i < 8; ++i) any |= cnt[i] < 32u;
        if (any) {
          unsigned rank = __hip_atomic_fetch_add(ctrl + C_SPARE / 4, 1u, __ATOMIC_RELAXED, __HIP_MEMORY_SCOPE_AGENT);
#pragma unroll
          for (int i = 0; i < 8; ++i)
            for (unsigned u = cnt[i]; u < 32u; ++u) {
              const int r = role_of((unsigned)i, u);
              if (r >= 0) { if (rank == 0u && role < 0) role = r; --rank; }
            }
        }
      } else {
        __hip_atomic_store(ctrl + C_ERR / 4, 1u, __ATOMIC_RELAXED, __HIP_MEMORY_SCOPE_AGENT);
      }
    }
    *(LAS int*)L = role;
  }
  __syncthreads();
  role = __builtin_amdgcn_readfirstlane(*(LAS int*)L);
  }
  __syncthreads();
  if (role < 0) return;
  if (role < 204) STAMP(role, 0);
  if (role < 64) role_rec(p, wb, rw, role, L, tid);
  if (role >= 204) { role_warm(wb, rw, role - 204, L, tid); return; }
  if (role >= 144 || (role >= 96 && role < 112)) {
    const unsigned long long t0 = __builtin_amdgcn_s_memrealtime();
    while (__builtin_amdgcn_s_memrealtime() - t0 < (role < 112 ? LATE_Q : LATE_S)) __builtin_amdgcn_s_sleep(8);
  }
  if (role >= 96 && role < 144) role_qkv(p, wb, rw, role - 96, L, tid);
  if (role < 80) role_red1(p, wb, rw, role - 64, L, tid);
  else if (role < 96) role_red2(p, wb, rw, role - 80, tid);
  else if (role < 176) role_att(p, wb, rw, role - 144, L, tid);
  else role_ffn(p, wb, rw, role - 176, L, tid);
}

extern "C" void kernel_launch(void* const* d_in, const int* in_sizes, int n_in, void* d_out, int out_size, void* d_ws, size_t ws_size,
                              hipStream_t stream) {
  Params p;
  p.x = (const uint16_t*)d_in[0]; p.mem0 = (const uint16_t*)d_in[1];
  p.wq = (const uint16_t*)d_in[2]; p.wk = (const uint16_t*)d_in[3]; p.wv = (const uint16_t*)d_in[4]; p.wo = (const uint16_t*)d_in[5];
  p.w1 = (const uint16_t*)d_in[8]; p.b1 = (const uint16_t*)d_in[9]; p.w2 = (const uint16_t*)d_in[10]; p.b2 = (const uint16_t*)d_in[11];
  p.c = (const uint16_t*)d_in[12]; p.wmod = (const uint16_t*)d_in[13];
  p.out = (uint16_t*)d_out; p.ws = (char*)d_ws;
  (void)hipFuncSetAttribute((const void*)k_main, hipFuncAttributeMaxDynamicSharedMemorySize, LDS_BYTES);
  k_setup<<<72 * 8, 256, 0, stream>>>(p);
  k_main<<<256, 256, LDS_BYTES, stream>>>(p);
}
```
